```python
import torch

def _softmax(x, axis=-1):
    return torch.softmax(x, dim=axis)
def _gelu(x):
    return torch.nn.functional.gelu(x, approximate="tanh")
def _silu(x):
    return torch.nn.functional.silu(x)
def _sigmoid(x):
    return torch.sigmoid(x)
def _layernorm(x, axis=-1, eps=1e-5):
    m = x.mean(dim=axis, keepdim=True)
    v = x.var(dim=axis, keepdim=True, unbiased=False)
    return (x - m) / torch.sqrt(v + eps)
def _scatter_add(tgt, idx, src, axis=0):
    return tgt.clone().index_add_(axis, idx, src)
def _segment_sum(src, seg, n):
    out = torch.zeros((n,) + src.shape[1:], dtype=src.dtype)
    return out.index_add_(0, seg, src)
def _pad(x, pw):
    flat = tuple(v for pair in reversed(pw) for v in pair)
    return torch.nn.functional.pad(x, flat)

kernel_name = 'interleave_layers__sliding_block_cached__glu_gated_swiglu__decode'

def setup_inputs():

    _w = (lambda a: (lambda t: t.to(torch.bfloat16) if t.is_floating_point() else t)(torch.as_tensor(a)))
    def _A_setup():
        B, Sq, D = 1, 1, 448
        Hq, Hkv, Sk, Dh = 16, 8, 8192, 256
        assert Hq % Hkv == 0, (Hq, Hkv)
        return {
            "x": _w((lambda *s: torch.randn(*s, dtype=torch.bfloat16) * 0.05)(B, Sq, D)),
            "k_cache": _w((lambda *s: torch.randn(*s, dtype=torch.bfloat16) * 1.6)(B, Hkv, Sk, Dh)),
            "v_cache": _w((lambda *s: torch.randn(*s, dtype=torch.bfloat16) * 0.8)(B, Hkv, Sk, Dh)),
            "wq": _w((lambda *s: torch.randn(*s, dtype=torch.bfloat16) * 1.6)(D, Hq * Dh) * (D ** -0.5)),
            "wo": _w((lambda *s: torch.randn(*s, dtype=torch.bfloat16) * 0.8)(Hq * Dh, D) * ((Hq * Dh) ** -0.5)),
        }
    _A_in = _A_setup()

    def _B_setup():
        B, S, D, F = 1, 1, 448, 1792
        return {
            "x": _w((lambda *s: torch.randn(*s, dtype=torch.bfloat16) * 0.05)(B, S, D)),
            "w1": _w((lambda *s: torch.randn(*s, dtype=torch.bfloat16) * 0.4)(D, F) * (2.0 / D) ** 0.5),
            "w3": _w((lambda *s: torch.randn(*s, dtype=torch.bfloat16) * 0.4)(D, F) * (2.0 / D) ** 0.5),
            "w2": _w((lambda *s: torch.randn(*s, dtype=torch.bfloat16) * 0.4)(F, D) * (2.0 / F) ** 0.5),
        }
    _B_in = _B_setup()

    return {
        "A_x": _A_in["x"],
        "A_k_cache": _A_in["k_cache"],
        "A_v_cache": _A_in["v_cache"],
        "A_wq": _A_in["wq"],
        "A_wo": _A_in["wo"],
        "B_x": _B_in["x"],
        "B_w1": _B_in["w1"],
        "B_w3": _B_in["w3"],
        "B_w2": _B_in["w2"],
    }

def reference(A_x, A_k_cache, A_v_cache, A_wq, A_wo, B_x, B_w1, B_w3, B_w2):
    def _A_ref(x, k_cache, v_cache, wq, wo):
        B, Sq, D = x.shape
        Hq, Dh = 16, 256
        Hkv, Sk = k_cache.shape[1], k_cache.shape[2]
        W = min(4096, max(256, Sk // 4))
        rep = Hq // Hkv
        q = (lambda x, *d: x.permute(*d))((x @ wq).reshape(B, Sq, Hq, Dh), 0, 2, 1, 3)
        k = (lambda x, n, axis: torch.repeat_interleave(x, n, dim=axis))(k_cache[:, :, Sk - W:, :], rep, axis=1)
        v = (lambda x, n, axis: torch.repeat_interleave(x, n, dim=axis))(v_cache[:, :, Sk - W:, :], rep, axis=1)
        scores = torch.einsum("bhqd,bhkd->bhqk", q, k) * (Dh ** -0.5)
        p = _softmax(scores, axis=-1)
        o = torch.einsum("bhqk,bhkd->bhqd", p, v)
        return (lambda x, *d: x.permute(*d))(o, 0, 2, 1, 3).reshape(B, Sq, Hq * Dh) @ wo

    def _B_ref(x, w1, w3, w2):
        gate = torch.einsum("bsd,df->bsf", x, w1)
        up = torch.einsum("bsd,df->bsf", x, w3)
        h = _silu(gate) * up
        out = torch.einsum("bsf,fd->bsd", h, w2)
        return out

    x = A_x
    x = x + _A_ref(_layernorm(x, axis=-1), A_k_cache, A_v_cache, A_wq, A_wo)
    x = x + _B_ref(_layernorm(x, axis=-1), B_w1, B_w3, B_w2)
    x = x + _A_ref(_layernorm(x, axis=-1), A_k_cache, A_v_cache, A_wq, A_wo)
    x = x + _B_ref(_layernorm(x, axis=-1), B_w1, B_w3, B_w2)
    return x
```

```cpp
#include <hip/hip_runtime.h>
#include <hip/hip_bf16.h>
#include <stdint.h>

typedef unsigned short u16;
constexpr int D = 448, HQ = 16, HKV = 8, SK = 8192, DH = 256, W = 2048, F = 1792;

__device__ __forceinline__ float bf2f(u16 b) { return __uint_as_float((unsigned)b << 16); }
__device__ __forceinline__ float rbf(float f) {
  unsigned u = __float_as_uint(f); u += 0x7fffu + ((u >> 16) & 1u); return __uint_as_float(u & 0xffff0000u); }
__device__ __forceinline__ u16 f2bf(float f) { unsigned u = __float_as_uint(f); u += 0x7fffu + ((u >> 16) & 1u); return (u16)(u >> 16); }

__device__ void layernorm_lds(const float* x, float* out, float* red, int tid, int nthr) {
  float s = 0.f;
  for (int i = tid; i < D; i += nthr) s += x[i];
  for (int o = 32; o; o >>= 1) s += __shfl_xor(s, o);
  if ((tid & 63) == 0) red[tid >> 6] = s;
  __syncthreads();
  float tot = 0.f; for (int w = 0; w < nthr / 64; ++w) tot += red[w];
  const float mean = tot / D;
  __syncthreads();
  float v = 0.f;
  for (int i = tid; i < D; i += nthr) { float d = x[i] - mean; v += d * d; }
  for (int o = 32; o; o >>= 1) v += __shfl_xor(v, o);
  if ((tid & 63) == 0) red[tid >> 6] = v;
  __syncthreads();
  float vt = 0.f; for (int w = 0; w < nthr / 64; ++w) vt += red[w];
  const float var = rbf(vt / D), m = rbf(mean);
  const float den = rbf(sqrtf(rbf(var + 1e-5f)));
  for (int i = tid; i < D; i += nthr) out[i] = rbf(rbf(x[i] - m) / den);
  __syncthreads();
}

__global__ void k_q(const u16* xb, const float* xf, const u16* wq, float* q) {
  __shared__ float xs[D], ln[D], red[8], part[16][17];
  const int tid = threadIdx.x;
  for (int i = tid; i < D; i += 256) xs[i] = xb ? bf2f(xb[i]) : xf[i];
  __syncthreads();
  layernorm_lds(xs, ln, red, tid, 256);
  const int c = tid & 15, r = tid >> 4;
  float acc = 0.f;
  for (int k = r; k < D; k += 16) acc += ln[k] * bf2f(wq[(size_t)k * 4096 + blockIdx.x * 16 + c]);
  part[r][c] = acc;
  __syncthreads();
  if (tid < 16) { float s = 0.f; for (int i = 0; i < 16; ++i) s += part[i][tid]; q[blockIdx.x * 16 + tid] = rbf(s); }
}

__global__ void k_attn(const u16* K, const u16* V, const float* q, float* pm, float* po) {
  __shared__ float qs[512], sc[2][64], p[2][64], ml[2][2];
  const int tid = threadIdx.x, g = blockIdx.x >> 5, ch = blockIdx.x & 31;
  for (int i = tid; i < 512; i += 256) qs[i] = q[g * 512 + i];
  __syncthreads();
  const size_t base = ((size_t)g * SK + (SK - W) + ch * 64) * DH;
  if (tid < 128) { const int h = tid >> 6, key = tid & 63; float s = 0.f;
    for (int d = 0; d < DH; ++d) s += qs[h * 256 + d] * bf2f(K[base + (size_t)key * DH + d]);
    sc[h][key] = rbf(s) * 0.0625f; }
  __syncthreads();
  if (tid < 128) { const int h = tid >> 6, key = tid & 63; float m = sc[h][key];
    for (int o = 32; o; o >>= 1) m = fmaxf(m, __shfl_xor(m, o));
    float e = __expf(sc[h][key] - m); p[h][key] = e; float l = e;
    for (int o = 32; o; o >>= 1) l += __shfl_xor(l, o);
    if (key == 0) { ml[h][0] = m; ml[h][1] = l; } }
  __syncthreads();
  float a0 = 0.f, a1 = 0.f;
  for (int key = 0; key < 64; ++key) { const float v = bf2f(V[base + (size_t)key * DH + tid]); a0 += p[0][key] * v; a1 += p[1][key] * v; }
  po[((size_t)blockIdx.x * 2 + 0) * 256 + tid] = a0;
  po[((size_t)blockIdx.x * 2 + 1) * 256 + tid] = a1;
  if (tid < 4) pm[blockIdx.x * 4 + tid] = ml[tid >> 1][tid & 1];
}

__global__ void k_wo(const float* pm, const float* po, const u16* wo, float* part) {
  __shared__ float wgt[32], os[16];
  const int tid = threadIdx.x, g = blockIdx.x >> 5, j = blockIdx.x & 31, h = j >> 4, c0 = (j & 15) * 16;
  if (tid < 32) { float m = pm[(g * 32 + tid) * 4 + h * 2], l = pm[(g * 32 + tid) * 4 + h * 2 + 1];
    float M = m; for (int o = 16; o; o >>= 1) M = fmaxf(M, __shfl_xor(M, o, 32));
    float e = __expf(m - M); float L = e * l; for (int o = 16; o; o >>= 1) L += __shfl_xor(L, o, 32);
    wgt[tid] = e / L; }
  __syncthreads();
  if (tid < 16) { float s = 0.f; for (int i = 0; i < 32; ++i) s += wgt[i] * po[((size_t)(g * 32 + i) * 2 + h) * 256 + c0 + tid]; os[tid] = rbf(s); }
  __syncthreads();
  const int row0 = (g * 2 + h) * 256 + c0;
  for (int c = tid; c < D; c += 256) { float s = 0.f; for (int i = 0; i < 16; ++i) s += os[i] * bf2f(wo[(size_t)(row0 + i) * D + c]); part[(size_t)blockIdx.x * D + c] = s; }
}

__global__ void k_red(const u16* xb, const float* xf, const float* part, float* xo, u16* outb) {
  __shared__ float red[4][64];
  const int tid = threadIdx.x, c = blockIdx.x * 64 + (tid & 63), r = tid >> 6;
  float s = 0.f;
  for (int i = r; i < 256; i += 4) s += part[(size_t)i * D + c];
  red[r][tid & 63] = s;
  __syncthreads();
  if (tid < 64) { float t = red[0][tid] + red[1][tid] + red[2][tid] + red[3][tid];
    float xi = xb ? bf2f(xb[c]) : xf[c]; float y = rbf(xi + rbf(t));
    if (xo) xo[c] = y; if (outb) outb[c] = f2bf(y); }
}

__global__ void k_mlp(const float* xf, const u16* w1, const u16* w3, const u16* w2, float* part) {
  __shared__ float xs[D], ln[D], red[8], pg[7][32], pu[7][32], hs[7];
  const int tid = threadIdx.x;
  for (int i = tid; i < D; i += 256) xs[i] = xf[i];
  __syncthreads();
  layernorm_lds(xs, ln, red, tid, 256);
  const int c = tid & 7, r = tid >> 3;
  if (c < 7) { float g = 0.f, u = 0.f;
    for (int k = r; k < D; k += 32) { g += ln[k] * bf2f(w1[(size_t)k * F + blockIdx.x * 7 + c]); u += ln[k] * bf2f(w3[(size_t)k * F + blockIdx.x * 7 + c]); }
    pg[c][r] = g; pu[c][r] = u; }
  __syncthreads();
  if (tid < 7) { float g = 0.f, u = 0.f; for (int i = 0; i < 32; ++i) { g += pg[tid][i]; u += pu[tid][i]; }
    g = rbf(g); u = rbf(u); float s = rbf(g / (1.f + __expf(-g))); hs[tid] = rbf(s * u); }
  __syncthreads();
  for (int cc = tid; cc < D; cc += 256) { float s = 0.f; for (int i = 0; i < 7; ++i) s += hs[i] * bf2f(w2[(size_t)(blockIdx.x * 7 + i) * D + cc]); part[(size_t)blockIdx.x * D + cc] = s; }
}

extern "C" void kernel_launch(void* const* d_in, const int* in_sizes, int n_in,
                              void* d_out, int out_size, void* d_ws, size_t ws_size,
                              hipStream_t stream) {
  const u16* x = (const u16*)d_in[0]; const u16* K = (const u16*)d_in[1]; const u16* V = (const u16*)d_in[2];
  const u16* wq = (const u16*)d_in[3]; const u16* wo = (const u16*)d_in[4];
  const u16* w1 = (const u16*)d_in[6]; const u16* w3 = (const u16*)d_in[7]; const u16* w2 = (const u16*)d_in[8];
  float* ws = (float*)d_ws;
  float* q = ws; float* pm = ws + 4096; float* po = pm + 1024; float* part = po + 256 * 512; float* x1 = part + 256 * D; float* x2 = x1 + 512; float* x3 = x2 + 512;
  u16* out = (u16*)d_out;
  for (int l = 0; l < 2; ++l) {
    const u16* xb = l == 0 ? x : nullptr; const float* xf = l == 0 ? nullptr : x2;
    k_q<<<256, 256, 0, stream>>>(xb, xf, wq, q);
    k_attn<<<256, 256, 0, stream>>>(K, V, q, pm, po);
    k_wo<<<256, 256, 0, stream>>>(pm, po, wo, part);
    float* xa = l == 0 ? x1 : x3;
    k_red<<<7, 256, 0, stream>>>(xb, xf, part, xa, nullptr);
    k_mlp<<<256, 256, 0, stream>>>(xa, w1, w3, w2, part);
    k_red<<<7, 256, 0, stream>>>(nullptr, xa, part, l == 0 ? x2 : nullptr, l == 0 ? nullptr : out);
  }
}
```

```cpp
#include <hip/hip_runtime.h>
#include <hip/hip_bf16.h>
#include <stdint.h>

#define DI __device__ __forceinline__
#define LAS __attribute__((address_space(3)))
typedef unsigned u32;
typedef unsigned long long u64;
typedef u32 v2u __attribute__((ext_vector_type(2)));
typedef u32 v4u __attribute__((ext_vector_type(4)));
typedef u32 v8u __attribute__((ext_vector_type(8)));
typedef float f4 __attribute__((ext_vector_type(4)));
typedef __bf16 bf2 __attribute__((ext_vector_type(2)));
typedef __bf16 bf4 __attribute__((ext_vector_type(4)));
typedef __bf16 bf8 __attribute__((ext_vector_type(8)));
typedef __bf16 bf16v __attribute__((ext_vector_type(16)));
typedef LAS char* lds_t;
typedef __amdgpu_buffer_rsrc_t rsrc_t;

constexpr int D = 448, SK = 8192, DH = 256, WIN = 2048, FF = 1792;
constexpr int NTHR = 512;
constexpr int JB = 0, JA = 8, JM = 24;
constexpr int NM = 28;
constexpr int NWG = 220;
constexpr int WS_E = 0;
constexpr int WS_RDY = 1024;
constexpr int WS_Q = 4096;
constexpr int WS_ST = WS_Q + 2 * 8 * 2048;
constexpr int WS_X1 = WS_ST + 2 * 8 * 256;
constexpr int X1_L = 8 * D * 4;
constexpr int WS_MP = 73728;
constexpr int MP_L = 29 * D * 4;
constexpr int WS_O = 196608;
constexpr int O_L = 8 * 16 * 2048;
constexpr int WS_END = WS_O + 2 * O_L;
constexpr int WS_DBG = 1 << 20;
static_assert(WS_X1 + 2 * X1_L <= WS_MP && WS_MP + 2 * MP_L <= WS_O && MP_L % 128 == 0, "ws map");
constexpr int L_IMG0 = 0;
constexpr int L_IMG1 = 57344;
constexpr int L_SM = 122880;
constexpr int L_XA = L_SM;
constexpr int L_ONE = L_SM + 1024;
constexpr int L_ZERO = L_SM + 2048;
constexpr int L_OA = L_SM + 2560;
constexpr int L_STAT = L_SM + 3584;
constexpr int LDS_BYTES = L_SM + 4096;
constexpr u32 SENT = 0xffffffffu;
constexpr int SPIN_MAX = 1 << 13;
constexpr int AUX_SC1 = 16, AUX_NT = 2;
#define LD_AUX 2

#define STAMP(i) do {} while (0)

DI rsrc_t mk_rsrc(const void* p, u32 bytes) { return __builtin_amdgcn_make_buffer_rsrc((void*)p, 0, bytes, 0x00020000); }
DI void fence_c() { asm volatile("" ::: "memory"); }
DI void bar() { asm volatile("s_waitcnt lgkmcnt(0)\n\ts_barrier" ::: "memory"); }
DI float u2f(u32 x) { return __builtin_bit_cast(float, x); }
DI u32 f2u(float x) { return __builtin_bit_cast(u32, x); }
DI u32 pk_bf16(float lo, float hi) { bf2 v = {(__bf16)lo, (__bf16)hi}; return __builtin_bit_cast(u32, v); }
DI float rbf(float x) { return u2f(pk_bf16(x, x) & 0xffff0000u); }
DI v8u cat8(v4u a, v4u b) { return __builtin_shufflevector(a, b, 0, 1, 2, 3, 4, 5, 6, 7); }
DI f4 smf(v4u a, v8u b, f4 c, int idx) {
  return __builtin_amdgcn_smfmac_f32_16x16x64_bf16(__builtin_bit_cast(bf8, a), __builtin_bit_cast(bf16v, b), c, idx, 0, 0);
}
template <int CTRL> DI float dpp(float x) { return u2f(__builtin_amdgcn_mov_dpp(f2u(x), CTRL, 0xf, 0xf, true)); }
constexpr int XOR1 = 0xB1, XOR2 = 0x4E, HMIR = 0x141, ROR8 = 0x128;
DI float row_sum16(float x) { x += dpp<XOR1>(x); x += dpp<XOR2>(x); x += dpp<HMIR>(x); x += dpp<ROR8>(x); return x; }
DI float row_max16(float x) { x = fmaxf(x, dpp<XOR1>(x)); x = fmaxf(x, dpp<XOR2>(x)); x = fmaxf(x, dpp<HMIR>(x)); x = fmaxf(x, dpp<ROR8>(x)); return x; }
DI float xrow_sum(float x) {
  auto a = __builtin_amdgcn_permlane16_swap(f2u(x), f2u(x), false, false); x = u2f(a[0]) + u2f(a[1]);
  auto b = __builtin_amdgcn_permlane32_swap(f2u(x), f2u(x), false, false); return u2f(b[0]) + u2f(b[1]);
}
DI float xrow_max(float x) {
  auto a = __builtin_amdgcn_permlane16_swap(f2u(x), f2u(x), false, false); x = fmaxf(u2f(a[0]), u2f(a[1]));
  auto b = __builtin_amdgcn_permlane32_swap(f2u(x), f2u(x), false, false); return fmaxf(u2f(b[0]), u2f(b[1]));
}
DI float red4(f4 v) {
  auto a = __builtin_amdgcn_permlane16_swap(f2u(v.x), f2u(v.y), false, false); const float u = u2f(a[0]) + u2f(a[1]);
  auto b = __builtin_amdgcn_permlane16_swap(f2u(v.z), f2u(v.w), false, false); const float t = u2f(b[0]) + u2f(b[1]);
  auto c = __builtin_amdgcn_permlane32_swap(f2u(u), f2u(t), false, false); return u2f(c[0]) + u2f(c[1]);
}
DI float dot2(u32 a, u32 b, float c) { return __builtin_amdgcn_fdot2_f32_bf16(__builtin_bit_cast(bf2, a), __builtin_bit_cast(bf2, b), c, false); }
DI bool any_nan(float x) { return __builtin_amdgcn_ballot_w64(x != x) != 0ull; }
DI v4u lds128(lds_t L, int off) { return *(LAS v4u*)(L + off); }
DI v2u ldtr(lds_t L, int off) { return __builtin_bit_cast(v2u, __builtin_amdgcn_ds_read_tr16_b64_v4bf16((LAS bf4*)(L + off))); }
DI void put_A(lds_t L, int base, int blk, int lane, float x) {
  auto s = __builtin_amdgcn_permlane16_swap(f2u(x), f2u(x), false, false);
  const u32 d = pk_bf16(u2f(s[0]), u2f(s[1]));
  const int q4 = lane & 15;
  *(LAS u32*)(L + base + (32 * blk + 8 * (q4 >> 2) + 4 * (lane >> 5) + (q4 & 3)) * 4) = d;
}
template <int N> DI void stage128(rsrc_t rs, u32 goff, u32 rstride, lds_t L, int lbase, int n0, int lane) {
#pragma unroll
  for (int u = 0; u < N; ++u) {
    const int n = n0 + u;
    const u32 chunk = (lane & 7) ^ ((((lane >> 4) & 1) << 1) | ((n & 1) << 2));
    const u32 voff = goff + (u32)(lane >> 3) * rstride + chunk * 16;
    __builtin_amdgcn_raw_ptr_buffer_load_lds(rs, (LAS void*)(L + lbase + n * 1024), 16, voff, (u32)n * 8u * rstride, 0, LD_AUX);
  }
}
DI int tr_base128(int t, int lane) {
  const int gb = lane >> 4, q = (lane >> 2) & 3, p = lane & 3;
  const int g = (((q >> 1) & 1) << 1) | ((gb & 1) << 2);
  return (8 * gb + q) * 128 + (((2 * t + (p >> 1)) ^ g) * 16) + 8 * (p & 1);
}
template <int OFF> DI v8u frag128(lds_t L, int base) {
  const v2u a = ldtr(L, base + OFF), b = ldtr(L, base + OFF + 512), c = ldtr(L, base + OFF + 4096), d = ldtr(L, base + OFF + 4608);
  const v8u r = {a.x, a.y, b.x, b.y, c.x, c.y, d.x, d.y};
  return r;
}
DI void ln_stats(lds_t L, int lane, float& mean, float& rstd) {
  const v4u v = lds128(L, lane < 56 ? L_XA + lane * 16 : L_ZERO);
  float s1 = 0.f, s2 = 0.f;
  s1 = dot2(v.x, 0x3f803f80u, s1); s2 = dot2(v.x, v.x, s2);
  s1 = dot2(v.y, 0x3f803f80u, s1); s2 = dot2(v.y, v.y, s2);
  s1 = dot2(v.z, 0x3f803f80u, s1); s2 = dot2(v.z, v.z, s2);
  s1 = dot2(v.w, 0x3f803f80u, s1); s2 = dot2(v.w, v.w, s2);
  auto a = __builtin_amdgcn_permlane16_swap(f2u(s1), f2u(s2), false, false); float z = u2f(a[0]) + u2f(a[1]);
  auto b = __builtin_amdgcn_permlane32_swap(f2u(z), f2u(z), false, false); z = u2f(b[0]) + u2f(b[1]);
  z = row_sum16(z);
  auto c = __builtin_amdgcn_permlane16_swap(f2u(z), f2u(z), false, false);
  mean = u2f(c[0]) * (1.0f / D);
  const float var = fmaxf(u2f(c[1]) * (1.0f / D) - mean * mean, 0.f);
  rstd = __builtin_amdgcn_rsqf(var + 1e-5f);
}
DI void wait_ready(rsrc_t ws, u64 T, int lane) {
  const u32 off = WS_RDY + (lane < NWG / 4 ? lane : 0) * 32;
  for (int spin = 0; spin < SPIN_MAX; ++spin) {
    fence_c();
    const v4u a = __builtin_amdgcn_raw_buffer_load_b128(ws, off, 0, AUX_SC1), b = __builtin_amdgcn_raw_buffer_load_b128(ws, off + 16, 0, AUX_SC1);
    const u32 lo = (u32)T, hi = (u32)(T >> 32);
    const u32 bad = (a.x ^ lo) | (a.y ^ hi) | (a.z ^ lo) | (a.w ^ hi) | (b.x ^ lo) | (b.y ^ hi) | (b.z ^ lo) | (b.w ^ hi);
    if (__builtin_amdgcn_ballot_w64(bad != 0) == 0ull) break;
    __builtin_amdgcn_s_sleep(8);
  }
  fence_c();
}
template <int NR, bool EXTRA> DI float gather_rows(rsrc_t ws, u32 base, int blk, int lane, float& xv) {
  constexpr int NI = (NR + 3) / 4;
  const int ps = lane >> 4;
  const u32 off = base + ps * (D * 4) + blk * 256 + (lane & 15) * 16;
  const u32 xoff = base + NR * (D * 4) + (blk * 64 + 4 * (lane & 15) + ps) * 4;
  float r = 0.f;
  for (int spin = 0; spin < SPIN_MAX; ++spin) {
    fence_c();
    v4u v[NI];
#pragma unroll
    for (int i = 0; i < NI; ++i) v[i] = __builtin_amdgcn_raw_buffer_load_b128(ws, off, i * 4 * (D * 4), AUX_SC1);
    u32 xr = 0;
    if (EXTRA) xr = __builtin_amdgcn_raw_buffer_load_b32(ws, xoff, 0, AUX_SC1);
    f4 acc = __builtin_bit_cast(f4, v[0]);
#pragma unroll
    for (int i = 1; i < NI; ++i) {
      if (4 * i + 3 < NR) acc += __builtin_bit_cast(f4, v[i]);
      else if (4 * i + ps < NR) acc += __builtin_bit_cast(f4, v[i]);
    }
    r = red4(acc);
    xv = u2f(xr);
    if (!any_nan(EXTRA ? r + xv : r)) break;
  }
  fence_c();
  return r;
}

DI void role_A(const void* Kp, const void* Vp, char* wsp, lds_t L, int g, int sp, int lane, int wave) {
  const rsrc_t ws = mk_rsrc(wsp, WS_END);
  const size_t kvoff = ((size_t)g * SK + (SK - WIN) + sp * 128) * (DH * 2);
  const rsrc_t rk = mk_rsrc((const char*)Kp + kvoff, 128 * 512), rv = mk_rsrc((const char*)Vp + kvoff, 128 * 512);
  const v2u ev = __builtin_amdgcn_raw_buffer_load_b64(ws, WS_E, 0, AUX_SC1);
  if (wave == 0) {
    const v4u s4 = {SENT, SENT, SENT, SENT};
#pragma unroll
    for (int l2 = 0; l2 < 2; ++l2) {
      const u32 o = WS_O + l2 * O_L + (g * 16 + sp) * 2048 + lane * 16;
      __builtin_amdgcn_raw_buffer_store_b128(s4, ws, o, 0, AUX_SC1);
      __builtin_amdgcn_raw_buffer_store_b128(s4, ws, o + 1024, 0, AUX_SC1);
    }
    if (lane < 4) { const v2u s2 = {SENT, SENT}; __builtin_amdgcn_raw_buffer_store_b64(s2, ws, WS_ST + (lane >> 1) * 2048 + g * 256 + (lane & 1) * 128 + sp * 8, 0, AUX_SC1); }
  }
  __builtin_amdgcn_sched_barrier(0);
  {
    const u32 hi = lane >> 5, sl = (lane & 31) ^ (2 * hi);
#pragma unroll
    for (int u = 0; u < 8; ++u) {
      const u32 voff = hi * 512 + ((sl ^ (4 * (u & 1) + 8 * (u >> 2))) * 16);
      __builtin_amdgcn_raw_ptr_buffer_load_lds(rv, (LAS void*)(L + L_IMG0 + (wave * 8 + u) * 1024), 16, voff, (u32)(wave * 8 + u) * 1024u, 0, LD_AUX);
    }
  }
  v8u kf[4];
  {
    const u32 off = (wave * 16 + (lane & 15)) * 512 + (lane >> 4) * 16;
#pragma unroll
    for (int s = 0; s < 4; ++s)
      kf[s] = cat8(__builtin_amdgcn_raw_buffer_load_b128(rk, off + s * 128, 0, LD_AUX), __builtin_amdgcn_raw_buffer_load_b128(rk, off + s * 128 + 64, 0, LD_AUX));
  }
  __builtin_amdgcn_sched_barrier(0);
  const u64 E = ((u64)(u32)__builtin_amdgcn_readfirstlane(ev.y) << 32) | (u32)__builtin_amdgcn_readfirstlane(ev.x);
  if (wave == 0) {
    asm volatile("s_waitcnt vmcnt(16)" ::: "memory");
    if (lane == 0) { const u64 T = E + 2; const v2u t = {(u32)T, (u32)(T >> 32)}; __builtin_amdgcn_raw_buffer_store_b64(t, ws, WS_RDY + blockIdx.x * 8, 0, AUX_SC1); }
  }
  if (wave == 1) { *(LAS u32*)(L + L_ZERO + lane * 4) = 0u; *(LAS u32*)(L + L_ZERO + 256 + lane * 4) = 0u; }
  STAMP(0);
  asm volatile("s_waitcnt vmcnt(0)" ::: "memory");
  bar();
  STAMP(1);
  v8u vf[2][2];
#pragma unroll
  for (int cc = 0; cc < 2; ++cc) {
    const int gb = lane >> 4, q = (lane >> 2) & 3, p = lane & 3;
    const int f = (q << 1) | ((gb & 1) << 3);
    const int base = L_IMG0 + (8 * gb + q) * 512 + (((4 * wave + 2 * cc + (p >> 1)) ^ f) * 16) + 8 * (p & 1);
#pragma unroll
    for (int t2 = 0; t2 < 2; ++t2) {
      const v2u a = ldtr(L, base + t2 * 32768), b = ldtr(L, base + t2 * 32768 + 2048), c = ldtr(L, base + t2 * 32768 + 16384), d = ldtr(L, base + t2 * 32768 + 18432);
      vf[cc][t2] = v8u{a.x, a.y, b.x, b.y, c.x, c.y, d.x, d.y};
    }
  }
  const int idx = (lane & 1) ? 0xEEEE : 0x4444;
  const u32 qoff = WS_Q + g * 2048 + (((lane >> 1) & 1) * 128 + 8 * (lane >> 4) + 4 * (lane & 1)) * 8;
  const int paddr = (((lane >> 2) & 3) == (lane >> 4)) ? L_OA + ((lane >> 4) * 16 + (lane & 3) * 4) * 4 : L_ZERO;
  constexpr float C2 = 0.0625f * 1.44269504088896f;
#pragma unroll 1
  for (int layer = 0; layer < 2; ++layer) {
    const u32 tag = (u32)E + 1 + layer;
    f4 d = {0.f, 0.f, 0.f, 0.f};
    for (int spin = 0; spin < SPIN_MAX; ++spin) {
      fence_c();
      v4u a[8];
#pragma unroll
      for (int s = 0; s < 4; ++s) {
        a[2 * s] = __builtin_amdgcn_raw_buffer_load_b128(ws, qoff + s * 256, layer * (8 * 2048), AUX_SC1);
        a[2 * s + 1] = __builtin_amdgcn_raw_buffer_load_b128(ws, qoff + s * 256 + 16, layer * (8 * 2048), AUX_SC1);
      }
      u32 bad = 0;
      d = f4{0.f, 0.f, 0.f, 0.f};
#pragma unroll
      for (int s = 0; s < 4; ++s) {
        d = smf(v4u{a[2 * s].x, a[2 * s].z, a[2 * s + 1].x, a[2 * s + 1].z}, kf[s], d, idx);
        bad |= (a[2 * s].y ^ tag) | (a[2 * s].w ^ tag) | (a[2 * s + 1].y ^ tag) | (a[2 * s + 1].w ^ tag);
      }
      if (__builtin_amdgcn_ballot_w64(bad != 0) == 0ull) break;
    }
    fence_c();
    STAMP(4 + layer * 8);
    const int h = (lane >> 4) & 1;
    const float raw = h ? d.z + d.w : d.x + d.y;
    const float t = rbf(raw) * C2;
    const float mt = row_max16(t);
    const float p = __builtin_amdgcn_exp2f(t - mt);
    const float lt = row_sum16(p);
    const float nb = dpp<XOR1>(p);
    if (lane < 32) {
      if ((lane & 1) == 0) *(LAS u32*)(L + L_OA + (wave * 16 + h * 8 + ((lane >> 1) & 1) * 4 + ((lane & 15) >> 2)) * 4) = pk_bf16(p, nb);
      if ((lane & 15) == 1) *(LAS v2u*)(L + L_STAT + (h * 8 + wave) * 8) = v2u{f2u(mt), f2u(lt)};
    }
    bar();
    STAMP(5 + layer * 8);
    const v4u pa0 = lds128(L, paddr), pa1 = lds128(L, paddr + 256);
    const v2u st = *(LAS v2u*)(L + L_STAT + (lane & 15) * 8);
    const int g4 = lane >> 4;
    const float m00 = *(LAS float*)(L + L_STAT + g4 * 8), m01 = *(LAS float*)(L + L_STAT + (4 + g4) * 8);
    const float m10 = *(LAS float*)(L + L_STAT + (8 + g4) * 8), m11 = *(LAS float*)(L + L_STAT + (12 + g4) * 8);
    const f4 z = {0.f, 0.f, 0.f, 0.f};
    const f4 d00 = smf(pa0, vf[0][0], z, idx), d10 = smf(pa0, vf[1][0], z, idx), d01 = smf(pa1, vf[0][1], z, idx), d11 = smf(pa1, vf[1][1], z, idx);
    float M = u2f(st.x);
    M = fmaxf(M, dpp<XOR1>(M)); M = fmaxf(M, dpp<XOR2>(M)); M = fmaxf(M, dpp<HMIR>(M));
    float Ls = __builtin_amdgcn_exp2f(u2f(st.x) - M) * u2f(st.y);
    Ls += dpp<XOR1>(Ls); Ls += dpp<XOR2>(Ls); Ls += dpp<HMIR>(Ls);
    const float Mo = dpp<ROR8>(M);
    const float M0 = (lane & 8) ? Mo : M, M1 = (lane & 8) ? M : Mo;
    const float f00 = __builtin_amdgcn_exp2f(m00 - M0), f01 = __builtin_amdgcn_exp2f(m01 - M0), f10 = __builtin_amdgcn_exp2f(m10 - M1), f11 = __builtin_amdgcn_exp2f(m11 - M1);
    f4 o;
    o.x = f00 * (d00.x + d00.y) + f01 * (d01.x + d01.y);
    o.y = f00 * (d10.x + d10.y) + f01 * (d11.x + d11.y);
    o.z = f10 * (d00.z + d00.w) + f11 * (d01.z + d01.w);
    o.w = f10 * (d10.z + d10.w) + f11 * (d11.z + d11.w);
    const float ov = red4(o);
    __builtin_amdgcn_raw_buffer_store_b32(f2u(ov), ws, WS_O + (g * 16 + sp) * 2048 + (lane >> 5) * 1024 + (32 * wave + (lane & 31)) * 4, layer * O_L, AUX_SC1);
    if (wave == 0 && (lane & 55) == 0)
      __builtin_amdgcn_raw_buffer_store_b64(v2u{f2u(M), f2u(Ls)}, ws, WS_ST + g * 256 + (lane >> 3) * 128 + sp * 8, layer * 2048, AUX_SC1);
    STAMP(6 + layer * 8);
  }
}

DI void role_B(const void* xp, const void* wqp, const void* wop, char* wsp, lds_t L, int g, int c, int lane, int wave) {
  const rsrc_t ws = mk_rsrc(wsp, WS_END);
  const rsrc_t rq = mk_rsrc(wqp, D * 4096 * 2), ro = mk_rsrc(wop, 4096 * D * 2), rx = mk_rsrc(xp, D * 2);
  const v2u ev = __builtin_amdgcn_raw_buffer_load_b64(ws, WS_E, 0, AUX_SC1);
  const int col = 4 * (lane & 15) + (lane >> 4);
  const u32 x0 = __builtin_amdgcn_raw_buffer_load_b16(rx, (wave < 7 ? wave * 64 + col : 0) * 2, 0, 0);
  if (wave == 0 && c < 7 && lane < 32) {
    const v4u s4 = {SENT, SENT, SENT, SENT};
    __builtin_amdgcn_raw_buffer_store_b128(s4, ws, WS_X1 + (lane >> 4) * X1_L + g * (D * 4) + c * 256 + (lane & 15) * 16, 0, AUX_SC1);
  }
  __builtin_amdgcn_sched_barrier(0);
  stage128<7>(rq, (g * 512 + c * 64) * 2, 8192, L, L_IMG0, wave * 7, lane);
  __builtin_amdgcn_sched_barrier(0);
  const u64 E = ((u64)(u32)__builtin_amdgcn_readfirstlane(ev.y) << 32) | (u32)__builtin_amdgcn_readfirstlane(ev.x);
  const u64 T = E + 2;
  if (wave == 0) {
    asm volatile("s_waitcnt vmcnt(7)" ::: "memory");
    if (lane == 0) { const v2u t = {(u32)T, (u32)(T >> 32)}; __builtin_amdgcn_raw_buffer_store_b64(t, ws, WS_RDY + blockIdx.x * 8, 0, AUX_SC1); }
  }
  if (wave == 7) {
#pragma unroll
    for (int i = 0; i < 4; ++i) *(LAS u32*)(L + L_ONE + (i * 64 + lane) * 4) = 0x3f803f80u;
    *(LAS u32*)(L + L_ZERO + lane * 4) = 0u;
  }
  STAMP(0);
  asm volatile("s_waitcnt vmcnt(0)" ::: "memory");
  bar();
  STAMP(1);
  v8u fr[8];
  const int trb = tr_base128(wave & 3, lane);
  if (wave < 4) {
    fr[0] = frag128<0>(L, L_IMG0 + trb); fr[1] = frag128<8192>(L, L_IMG0 + trb); fr[2] = frag128<2 * 8192>(L, L_IMG0 + trb); fr[3] = frag128<3 * 8192>(L, L_IMG0 + trb);
    fr[4] = frag128<4 * 8192>(L, L_IMG0 + trb); fr[5] = frag128<5 * 8192>(L, L_IMG0 + trb); fr[6] = frag128<6 * 8192>(L, L_IMG0 + trb);
    fr[7] = fr[6];
  } else {
#pragma unroll
    for (int i = 0; i < 8; ++i) fr[i] = v8u{0, 0, 0, 0, 0, 0, 0, 0};
  }
  const int idx = (lane & 1) ? 0xEEEE : 0x4444;
  const int aoff = (8 * (lane >> 4) + 4 * (lane & 1)) * 4;
  const int h = wave >> 2;
#pragma unroll 1
  for (int layer = 0; layer < 2; ++layer) {
    if (wave < 7) {
      float x;
      if (layer == 0) x = u2f(x0 << 16);
      else { float x1; const float s = gather_rows<NM, true>(ws, WS_MP, wave, lane, x1); x = rbf(x1 + rbf(s)); }
      put_A(L, L_XA, wave, lane, x);
    }
    STAMP(4 + layer * 8);
    bar();
    if (wave < 4) {
      const int ab = ((lane & 2) ? L_ONE : L_XA) + aoff;
      f4 d = {0.f, 0.f, 0.f, 0.f};
#pragma unroll
      for (int s = 0; s < 7; ++s) d = smf(lds128(L, ab + s * 128), fr[s], d, idx);
      float mean, rstd;
      ln_stats(L, lane, mean, rstd);
      const float q = rstd * ((d.x + d.y) - mean * (d.z + d.w));
      const float nb = dpp<XOR1>(q);
      if (lane < 16 && (lane & 1) == 0) {
        const int i = lane >> 1;
        const u32 pos = ((c >> 2) * 4 + (c & 3)) * 32 + 8 * wave + 4 * (i & 1) + (i >> 1);
        __builtin_amdgcn_raw_buffer_store_b64(v2u{pk_bf16(q, nb), (u32)E + 1 + layer}, ws, WS_Q + g * 2048 + pos * 8, layer * (8 * 2048), AUX_SC1);
      }
    }
    STAMP(5 + layer * 8);
    if (layer == 0) {
      if (c < 7) stage128<8>(ro, g * 512 * (D * 2) + c * 128, D * 2, L, L_IMG1, wave * 8, lane);
      wait_ready(ws, T, lane);
      asm volatile("s_waitcnt vmcnt(0)" ::: "memory");
      bar();
      if (wave >= 4 && c < 7) {
        fr[0] = frag128<0>(L, L_IMG1 + trb); fr[1] = frag128<8192>(L, L_IMG1 + trb); fr[2] = frag128<2 * 8192>(L, L_IMG1 + trb); fr[3] = frag128<3 * 8192>(L, L_IMG1 + trb);
        fr[4] = frag128<4 * 8192>(L, L_IMG1 + trb); fr[5] = frag128<5 * 8192>(L, L_IMG1 + trb); fr[6] = frag128<6 * 8192>(L, L_IMG1 + trb); fr[7] = frag128<7 * 8192>(L, L_IMG1 + trb);
      }
    }
    if (c == 7) continue;
    STAMP(6 + layer * 8);
    {
      const int ss = lane >> 4;
      const u32 so = WS_ST + layer * 2048 + g * 256 + h * 128 + ss * 8;
      const u32 oo = WS_O + layer * O_L + (g * 16 + ss) * 2048 + h * 1024 + (wave & 3) * 256 + (lane & 15) * 16;
      float ov = 0.f;
      for (int spin = 0; spin < SPIN_MAX; ++spin) {
        fence_c();
        v2u st[4]; v4u o[4];
#pragma unroll
        for (int i = 0; i < 4; ++i) st[i] = __builtin_amdgcn_raw_buffer_load_b64(ws, so, i * 32, AUX_SC1);
#pragma unroll
        for (int i = 0; i < 4; ++i) o[i] = __builtin_amdgcn_raw_buffer_load_b128(ws, oo, i * 4 * 2048, AUX_SC1);
        const float M = xrow_max(fmaxf(fmaxf(u2f(st[0].x), u2f(st[1].x)), fmaxf(u2f(st[2].x), u2f(st[3].x))));
        float e[4], Ls = 0.f;
#pragma unroll
        for (int i = 0; i < 4; ++i) { e[i] = __builtin_amdgcn_exp2f(u2f(st[i].x) - M); Ls += e[i] * u2f(st[i].y); }
        Ls = xrow_sum(Ls);
        f4 acc = e[0] * __builtin_bit_cast(f4, o[0]);
#pragma unroll
        for (int i = 1; i < 4; ++i) acc += e[i] * __builtin_bit_cast(f4, o[i]);
        ov = red4(acc) * __builtin_amdgcn_rcpf(Ls);
        if (!any_nan(ov)) break;
      }
      fence_c();
      put_A(L, L_OA, wave, lane, ov);
    }
    STAMP(7 + layer * 8);
    bar();
    if (wave >= 4) {
      f4 d = {0.f, 0.f, 0.f, 0.f};
#pragma unroll
      for (int s = 0; s < 8; ++s) d = smf(lds128(L, L_OA + aoff + s * 128), fr[s], d, idx);
      if (lane < 16) __builtin_amdgcn_raw_buffer_store_b32(f2u(d.x + d.y), ws, WS_X1 + g * (D * 4) + (c * 64 + (wave - 4) * 16 + lane) * 4, layer * X1_L, AUX_SC1);
    }
    STAMP(8 + layer * 8);
  }
}

DI void role_M(const void* xp, const void* w1p, const void* w3p, const void* w2p, void* outp, char* wsp, lds_t L, int m, int lane, int wave) {
  const rsrc_t ws = mk_rsrc(wsp, WS_END);
  const rsrc_t r1 = mk_rsrc(w1p, D * FF * 2), r3 = mk_rsrc(w3p, D * FF * 2), r2 = mk_rsrc(w2p, D * FF * 2), rx = mk_rsrc(xp, D * 2);
  const v2u ev = __builtin_amdgcn_raw_buffer_load_b64(ws, WS_E, 0, AUX_SC1);
  const int col = 4 * (lane & 15) + (lane >> 4);
  const u32 x0 = __builtin_amdgcn_raw_buffer_load_b16(rx, (wave < 7 ? wave * 64 + col : 0) * 2, 0, 0);
  if (wave == 0) {
    const v4u s4 = {SENT, SENT, SENT, SENT};
#pragma unroll
    for (int l2 = 0; l2 < 2; ++l2) {
      const u32 o = WS_MP + l2 * MP_L + m * (D * 4) + lane * 16;
      __builtin_amdgcn_raw_buffer_store_b128(s4, ws, o, 0, AUX_SC1);
      if (lane < 48) __builtin_amdgcn_raw_buffer_store_b128(s4, ws, o + 1024, 0, AUX_SC1);
      if (m == 0) {
        const u32 ox = WS_MP + l2 * MP_L + NM * (D * 4) + lane * 16;
        __builtin_amdgcn_raw_buffer_store_b128(s4, ws, ox, 0, AUX_SC1);
        if (lane < 48) __builtin_amdgcn_raw_buffer_store_b128(s4, ws, ox + 1024, 0, AUX_SC1);
      }
    }
  }
  __builtin_amdgcn_sched_barrier(0);
  stage128<7>(r1, m * 128, FF * 2, L, L_IMG0, wave * 7, lane);
  stage128<7>(r3, m * 128, FF * 2, L, L_IMG1, wave * 7, lane);
  __builtin_amdgcn_sched_barrier(0);
  const u64 E = ((u64)(u32)__builtin_amdgcn_readfirstlane(ev.y) << 32) | (u32)__builtin_amdgcn_readfirstlane(ev.x);
  const u64 T = E + 2;
  if (wave == 0) {
    asm volatile("s_waitcnt vmcnt(14)" ::: "memory");
    if (lane == 0) { const v2u t = {(u32)T, (u32)(T >> 32)}; __builtin_amdgcn_raw_buffer_store_b64(t, ws, WS_RDY + blockIdx.x * 8, 0, AUX_SC1); }
  }
  if (wave == 7) {
#pragma unroll
    for (int i = 0; i < 4; ++i) *(LAS u32*)(L + L_ONE + (i * 64 + lane) * 4) = 0x3f803f80u;
    *(LAS u32*)(L + L_ZERO + lane * 4) = 0u;
  }
  STAMP(0);
  asm volatile("s_waitcnt vmcnt(0)" ::: "memory");
  bar();
  STAMP(1);
  v8u fr[14];
  const int trb = tr_base128(wave & 3, lane);
  if (wave < 4) {
#define FR2(s) fr[s] = frag128<s * 8192>(L, L_IMG0 + trb); fr[7 + s] = frag128<s * 8192>(L, L_IMG1 + trb);
    FR2(0) FR2(1) FR2(2) FR2(3) FR2(4) FR2(5) FR2(6)
#undef FR2
  } else {
#pragma unroll
    for (int i = 0; i < 14; ++i) fr[i] = v8u{0, 0, 0, 0, 0, 0, 0, 0};
  }
  bar();
#pragma unroll
  for (int b = 0; b < 7; ++b) {
    const u32 chunk = (lane & 7) ^ ((((lane >> 4) & 1) << 1) | ((wave & 1) << 2));
    const u32 voff = (u32)(m * 64 + wave * 8 + (lane >> 3)) * (D * 2) + chunk * 16;
    __builtin_amdgcn_raw_ptr_buffer_load_lds(r2, (LAS void*)(L + L_IMG0 + (b * 8 + wave) * 1024), 16, voff, b * 128, 0, LD_AUX);
  }
  wait_ready(ws, T, lane);
  asm volatile("s_waitcnt vmcnt(0)" ::: "memory");
  bar();
  if (wave >= 4) {
#pragma unroll
    for (int u = 0; u < 7; ++u) {
      const int ct = 7 * (wave - 4) + u;
      fr[u] = frag128<0>(L, L_IMG0 + (ct >> 2) * 8192 + tr_base128(ct & 3, lane));
    }
  }
  STAMP(2);
  const int idx = (lane & 1) ? 0xEEEE : 0x4444;
  const int aoff = (8 * (lane >> 4) + 4 * (lane & 1)) * 4;
  float xres = u2f(x0 << 16);
#pragma unroll 1
  for (int layer = 0; layer < 2; ++layer) {
    if (wave < 7) {
      float dummy;
      const float a = gather_rows<8, false>(ws, WS_X1 + layer * X1_L, wave, lane, dummy);
      xres = rbf(xres + rbf(a));
      put_A(L, L_XA, wave, lane, xres);
      if (m == 0) __builtin_amdgcn_raw_buffer_store_b32(f2u(xres), ws, WS_MP + NM * (D * 4) + (wave * 64 + col) * 4, layer * MP_L, AUX_SC1);
    }
    STAMP(4 + layer * 8);
    bar();
    if (wave < 4) {
      const int ab = ((lane & 2) ? L_ONE : L_XA) + aoff;
      f4 dg = {0.f, 0.f, 0.f, 0.f}, du = {0.f, 0.f, 0.f, 0.f};
#pragma unroll
      for (int s = 0; s < 7; ++s) { const v4u a = lds128(L, ab + s * 128); dg = smf(a, fr[s], dg, idx); du = smf(a, fr[7 + s], du, idx); }
      float mean, rstd;
      ln_stats(L, lane, mean, rstd);
      const float gt = rbf(rstd * ((dg.x + dg.y) - mean * (dg.z + dg.w)));
      const float up = rbf(rstd * ((du.x + du.y) - mean * (du.z + du.w)));
      const float si = rbf(gt * __builtin_amdgcn_rcpf(1.0f + __builtin_amdgcn_exp2f(gt * -1.44269504088896f)));
      const float hv = si * up;
      const float nb = dpp<XOR1>(hv);
      if (lane < 16 && (lane & 1) == 0) { const int i = lane >> 1; *(LAS u32*)(L + L_OA + (8 * wave + 4 * (i & 1) + (i >> 1)) * 4) = pk_bf16(hv, nb); }
    }
    STAMP(5 + layer * 8);
    bar();
    if (wave >= 4) {
      const v4u a = lds128(L, L_OA + aoff);
      float y[7];
#pragma unroll
      for (int u = 0; u < 7; ++u) { const f4 d = smf(a, fr[u], f4{0.f, 0.f, 0.f, 0.f}, idx); y[u] = d.x + d.y; }
      const int g4 = lane >> 4;
      const float s0 = g4 == 0 ? y[0] : g4 == 1 ? y[1] : g4 == 2 ? y[2] : y[3];
      const float s1 = g4 == 0 ? y[4] : g4 == 1 ? y[5] : y[6];
      const u32 o = WS_MP + m * (D * 4) + (112 * (wave - 4) + lane) * 4;
      __builtin_amdgcn_raw_buffer_store_b32(f2u(s0), ws, o, layer * MP_L, AUX_SC1);
      if (lane < 48) __builtin_amdgcn_raw_buffer_store_b32(f2u(s1), ws, o + 256, layer * MP_L, AUX_SC1);
    }
    STAMP(6 + layer * 8);
    if (layer == 0) {
      if (wave < 7) { float dummy; const float s = gather_rows<NM, false>(ws, WS_MP, wave, lane, dummy); xres = rbf(xres + rbf(s)); }
    } else if (m < 7 && wave == m) {
      float dummy;
      const float s = gather_rows<NM, false>(ws, WS_MP + MP_L, wave, lane, dummy);
      const float y = rbf(xres + rbf(s));
      auto sw = __builtin_amdgcn_permlane16_swap(f2u(y), f2u(y), false, false);
      const u32 dw = pk_bf16(u2f(sw[0]), u2f(sw[1]));
      if ((lane & 16) == 0) ((u32*)outp)[32 * m + 2 * (lane & 15) + (lane >> 5)] = dw;
      if (m == 0 && lane == 0) __builtin_amdgcn_raw_buffer_store_b64(v2u{(u32)T, (u32)(T >> 32)}, ws, WS_E, 0, AUX_SC1);
    }
    STAMP(7 + layer * 8);
  }
}

__global__ void __launch_bounds__(NTHR, 2) fused_decode_kernel(const void* xp, const void* Kp, const void* Vp, const void* wqp, const void* wop,
                                                               const void* w1p, const void* w3p, const void* w2p, void* outp, char* wsp) {
  extern __shared__ __attribute__((aligned(16))) char smem[];
  const lds_t L = (lds_t)smem;
  const int lane = threadIdx.x & 63, wave = __builtin_amdgcn_readfirstlane(threadIdx.x >> 6);
  const int g = blockIdx.x & 7, j = blockIdx.x >> 3;
  if (j < JA) role_B(xp, wqp, wop, wsp, L, g, j, lane, wave);
  else if (j < JM) role_A(Kp, Vp, wsp, L, g, j - JA, lane, wave);
  else role_M(xp, w1p, w3p, w2p, outp, wsp, L, (j - JM) * 8 + g, lane, wave);
}

extern "C" void kernel_launch(void* const* d_in, const int* in_sizes, int n_in, void* d_out, int out_size, void* d_ws, size_t ws_size, hipStream_t stream) {
  (void)hipFuncSetAttribute((const void*)fused_decode_kernel, hipFuncAttributeMaxDynamicSharedMemorySize, LDS_BYTES);
  fused_decode_kernel<<<NWG, NTHR, LDS_BYTES, stream>>>(d_in[0], d_in[1], d_in[2], d_in[3], d_in[4], d_in[6], d_in[7], d_in[8], d_out, (char*)d_ws);
}
```

```cpp
#define T_W0 85
#define T_WV 80
#define T_A0 95
#define T_W1 103
#define T_M0 140
#define T_M1 124
#define T_B 118
#define T_A 118
#define T_F 30
#define D_RETRY 12
#define V_DELAY 15
#define H_LEAD 20
#define M_DELAY 100
#define WO_DELAY 0
#define K_DELAY 0
#define LD_AUX 2
#include <hip/hip_runtime.h>
#include <hip/hip_bf16.h>
#include <stdint.h>

#define DI __device__ __forceinline__
#define LAS __attribute__((address_space(3)))
typedef unsigned u32;
typedef unsigned long long u64;
typedef u32 v2u __attribute__((ext_vector_type(2)));
typedef u32 v4u __attribute__((ext_vector_type(4)));
typedef u32 v8u __attribute__((ext_vector_type(8)));
typedef float f4 __attribute__((ext_vector_type(4)));
typedef __bf16 bf2 __attribute__((ext_vector_type(2)));
typedef __bf16 bf4 __attribute__((ext_vector_type(4)));
typedef __bf16 bf8 __attribute__((ext_vector_type(8)));
typedef __bf16 bf16v __attribute__((ext_vector_type(16)));
typedef LAS char* lds_t;
typedef __amdgpu_buffer_rsrc_t rsrc_t;

constexpr int D = 448, SK = 8192, DH = 256, WIN = 2048, FF = 1792;
constexpr int NTHR = 512;
constexpr int JB = 0, JA = 8, JM = 24, JH = 28;
constexpr int NM = 28;
constexpr int NWG = 256;
constexpr int CP = 8192, CH = 16384;
constexpr int WS_E = 0;
constexpr int WS_RDY = 1024;
constexpr int BELL_C = 2048;
constexpr int WS_PROBE = 4096;
constexpr int WS_BELL = CH;
constexpr int BELL_M = WS_BELL, BELL_A0 = WS_BELL + 2048, BELL_R = WS_BELL + 3072, BELL_W = WS_BELL + 4096, BELL_D = WS_BELL + 6144;
constexpr int WS_Q = 2 * CH, Q_L = 2 * CH;
constexpr int WS_ST = WS_Q + 2 * Q_L, ST_L = 4096;
constexpr int WS_X1 = WS_ST + CH, X1_L = 2 * CH;
constexpr int WS_MP = WS_X1 + 2 * X1_L, MP_L = 8 * CH;
constexpr int WS_O = WS_MP + 2 * MP_L, O_L = 32 * CH;
constexpr int WS_END = WS_O + 2 * O_L;
constexpr int WS_DBG = 1 << 21;
static_assert(WS_END <= (1 << 21) - 256, "one 2-MB page");
#define PROBE_K 16
#define TUNE_DECL constexpr int tW0 = T_W0, tW1 = T_W1, tM0 = T_M0, tM1 = T_M1, tB = T_B, tA = T_A, tF = T_F, tML = M_DELAY, tA0 = T_A0, tWV = T_WV, tWO = WO_DELAY, tKD = K_DELAY, tVD = V_DELAY, tHL = H_LEAD
#define TUNE_USE (void)tW0; (void)tW1; (void)tM0; (void)tM1; (void)tB; (void)tA; (void)tF; (void)tML; (void)tA0; (void)tWV; (void)tWO; (void)tKD; (void)tVD; (void)tHL
#define D_POLL 6
constexpr int L_IMG0 = 0;
constexpr int L_IMG1 = 57344;
constexpr int L_SM = 131072;
constexpr int L_XA = L_SM;
constexpr int L_ONE = L_SM + 1024 + 128;
constexpr int L_ZERO = L_SM + 2048 + 128;
constexpr int L_OA = L_SM + 2816;
constexpr int L_STAT = L_SM + 3840;
constexpr int L_TGT = L_SM + 3904;
constexpr int L_DUM = L_SM + 3968;
constexpr int L_QP = L_SM + 4096;
constexpr int L_PF = L_SM + 8192;
constexpr int L_W2 = 114688;
constexpr int L_SM_M = 155648;
constexpr int LDS_BYTES = 163840;
constexpr u32 SENT = 0xffffffffu;
#define SPINLOG 13
constexpr int SPIN_MAX = 1 << SPINLOG;
constexpr int AUX_SC1 = 16;
#define WQ_AUX 0
#define WO_AUX 0
#define WM_AUX 0

#define STAMP_DECL do {} while (0)
#define STAMP(i) do {} while (0)
#define STAMP_L(i) do {} while (0)
#define RETRY(i, n) do {} while (0)
#define STAMP_OUT do {} while (0)

DI rsrc_t mk_rsrc(const void* p, u32 bytes) { return __builtin_amdgcn_make_buffer_rsrc((void*)p, 0, bytes, 0x00020000); }
DI void fence_c() { asm volatile("" ::: "memory"); }
DI void pin() { __builtin_amdgcn_sched_barrier(0); }
DI void bar() { asm volatile("s_waitcnt lgkmcnt(0)\n\ts_barrier" ::: "memory"); }
DI float u2f(u32 x) { return __builtin_bit_cast(float, x); }
DI u32 f2u(float x) { return __builtin_bit_cast(u32, x); }
DI u32 pk_bf16(float lo, float hi) { bf2 v = {(__bf16)lo, (__bf16)hi}; return __builtin_bit_cast(u32, v); }
DI float rbf(float x) { return u2f(pk_bf16(x, x) & 0xffff0000u); }
DI v8u cat8(v4u a, v4u b) { return __builtin_shufflevector(a, b, 0, 1, 2, 3, 4, 5, 6, 7); }
DI f4 smf(v4u a, v8u b, f4 c, int idx) {
  return __builtin_amdgcn_smfmac_f32_16x16x64_bf16(__builtin_bit_cast(bf8, a), __builtin_bit_cast(bf16v, b), c, idx, 0, 0);
}
template <int CTRL> DI float dpp(float x) { return u2f(__builtin_amdgcn_mov_dpp(f2u(x), CTRL, 0xf, 0xf, true)); }
constexpr int XOR1 = 0xB1, XOR2 = 0x4E, HMIR = 0x141, ROR8 = 0x128, NBC = 0x150;
#define MXD(ctl) "s_nop 1\n\tv_max_f32_dpp %0, %0, %0 " ctl " row_mask:0xf bank_mask:0xf\n\t"
DI float vmax(float a, float b) { float r; asm("v_max_f32 %0, %1, %2" : "=v"(r) : "v"(a), "v"(b)); return r; }
DI float max8(float x) { float r; asm("s_nop 1\n\tv_max_f32_dpp %0, %1, %1 quad_perm:[1,0,3,2] row_mask:0xf bank_mask:0xf\n\t" MXD("quad_perm:[2,3,0,1]") MXD("row_half_mirror") : "=&v"(r) : "v"(x)); return r; }
DI float row_max16(float x) { float r; asm("s_nop 1\n\tv_max_f32_dpp %0, %1, %1 quad_perm:[1,0,3,2] row_mask:0xf bank_mask:0xf\n\t" MXD("quad_perm:[2,3,0,1]") MXD("row_half_mirror") MXD("row_ror:8") : "=&v"(r) : "v"(x)); return r; }
DI float sum8(float x) { x += dpp<XOR1>(x); x += dpp<XOR2>(x); x += dpp<HMIR>(x); return x; }
DI float row_sum16(float x) { x = sum8(x); x += dpp<ROR8>(x); return x; }
DI float opq(float x) { asm("" : "+v"(x)); return x; }
template <class T> DI void keep(T& x) { asm volatile("" : "+v"(x)); }
DI float red4(float x, float y, float z, float w) {
  auto a = __builtin_amdgcn_permlane16_swap(f2u(x), f2u(y), false, false);
  auto b = __builtin_amdgcn_permlane16_swap(f2u(z), f2u(w), false, false);
  const float u = opq(u2f(a[0]) + u2f(a[1])), t = opq(u2f(b[0]) + u2f(b[1]));
  auto c = __builtin_amdgcn_permlane32_swap(f2u(u), f2u(t), false, false); return u2f(c[0]) + u2f(c[1]);
}
DI float dot2(u32 a, u32 b, float c) { return __builtin_amdgcn_fdot2_f32_bf16(__builtin_bit_cast(bf2, a), __builtin_bit_cast(bf2, b), c, false); }
DI bool any_nan(float x) { return __builtin_amdgcn_ballot_w64(x != x) != 0ull; }
DI v4u lds128(lds_t L, int off) { return *(LAS v4u*)(L + off); }
DI v2u ldtr(lds_t L, int off) { return __builtin_bit_cast(v2u, __builtin_amdgcn_ds_read_tr16_b64_v4bf16((LAS bf4*)(L + off))); }
DI u32 now32() { return (u32)__builtin_amdgcn_s_memrealtime(); }
DI void wait_until(u32 target) {
  int left = (int)(target - now32());
  if (left > 1500) return;
#pragma unroll 1
  while (left > 0) left = (int)(target - now32());
}
constexpr u32 OOB = 0x80000000u;
DI u32 put_A(lds_t L, int addr, float x) {
  auto s = __builtin_amdgcn_permlane16_swap(f2u(x), f2u(x), false, false);
  const u32 d = pk_bf16(u2f(s[0]), u2f(s[1]));
  *(LAS u32*)(L + addr) = d;
  return d;
}
DI float own_half(u32 d, int lane) { return u2f((lane & 16) ? d & 0xffff0000u : d << 16); }
DI int put_A_addr(int base, int blk, int lane) { const int q4 = lane & 15; return base + (32 * blk + 8 * (q4 >> 2) + 4 * (lane >> 5) + (q4 & 3)) * 4; }
template <int N, int AUX = LD_AUX> DI void stage128(rsrc_t rs, u32 goff, u32 rstride, lds_t L, int lbase, int n0, int lane) {
  const u32 v0 = goff + (u32)(lane >> 3) * rstride + (((lane & 7) ^ (((lane >> 4) & 1) << 1)) * 16), v1 = v0 ^ 64;
#pragma unroll
  for (int u = 0; u < N; ++u) {
    const int n = n0 + u;
    __builtin_amdgcn_raw_ptr_buffer_load_lds(rs, (LAS void*)(L + lbase + n * 1024), 16, (n & 1) ? v1 : v0, (u32)n * 8u * rstride, 0, AUX);
  }
}
DI int tr_base128(int t, int lane) {
  const int gb = lane >> 4, q = (lane >> 2) & 3, p = lane & 3;
  const int g = (((q >> 1) & 1) << 1) | ((gb & 1) << 2);
  return (8 * gb + q) * 128 + (((2 * t + (p >> 1)) ^ g) * 16) + 8 * (p & 1);
}
template <int OFF> DI v8u frag128(lds_t L, int base) {
  const v2u a = ldtr(L, base + OFF), b = ldtr(L, base + OFF + 512), c = ldtr(L, base + OFF + 4096), d = ldtr(L, base + OFF + 4608);
  const v8u r = {a.x, a.y, b.x, b.y, c.x, c.y, d.x, d.y};
  return r;
}
DI void ln_stats(v4u v, float& mean, float& rstd) {
  float s1 = dot2(v.x, 0x3f803f80u, 0.f), s2 = dot2(v.x, v.x, 0.f);
  s1 = dot2(v.y, 0x3f803f80u, s1); s2 = dot2(v.y, v.y, s2);
  s1 = dot2(v.z, 0x3f803f80u, s1); s2 = dot2(v.z, v.z, s2);
  s1 = dot2(v.w, 0x3f803f80u, s1); s2 = dot2(v.w, v.w, s2);
  auto a = __builtin_amdgcn_permlane16_swap(f2u(s1), f2u(s2), false, false); float z = u2f(a[0]) + u2f(a[1]);
  auto b = __builtin_amdgcn_permlane32_swap(f2u(z), f2u(z), false, false); z = u2f(b[0]) + u2f(b[1]);
  z = row_sum16(z) * (1.0f / D);
  auto c = __builtin_amdgcn_permlane16_swap(f2u(z), f2u(z), false, false);
  mean = u2f(c[0]);
  const float var = vmax(u2f(c[1]) - mean * mean, 0.f);
  rstd = __builtin_amdgcn_rsqf(var + 1e-5f);
}
DI void wait_ready(rsrc_t ws, u64 T, int lane) {
  const u32 off = WS_RDY + (lane < NM ? lane : 0) * 8;
#pragma unroll 1
  for (int spin = 0; spin < SPIN_MAX; ++spin) {
    fence_c();
    const v2u a = __builtin_amdgcn_raw_buffer_load_b64(ws, off, 0, AUX_SC1);
    if (__builtin_amdgcn_ballot_w64(((a.x ^ (u32)T) | (a.y ^ (u32)(T >> 32))) != 0u) == 0ull) break;
    __builtin_amdgcn_s_sleep(32);
  }
  fence_c();
}
template <int N> DI u32 bell_time(rsrc_t ws, u32 off, u32 tag, int lane) {
  const u32 o = off + ((lane & 15) < N ? (lane & 15) : 0) * 8;
  v2u v = {0u, 0u};
#pragma unroll 1
  for (int spin = 0; spin < SPIN_MAX * 2; ++spin) {
    fence_c();
    v = __builtin_amdgcn_raw_buffer_load_b64(ws, o, 0, AUX_SC1);
    if (__builtin_amdgcn_ballot_w64(v.x != tag) == 0ull) break;
    __builtin_amdgcn_s_sleep(D_POLL);
  }
  fence_c();
  const u32 t0 = __builtin_amdgcn_readfirstlane(v.y);
  const float r = row_max16((float)(int)(v.y - t0));
  return t0 + (u32)__builtin_amdgcn_readfirstlane((int)r);
}
DI u32 anchor_time(rsrc_t ws, u32 ta, u32 look, u32 tag, int lane) {
  wait_until(look);
  v2u v = {0u, 0u};
#pragma unroll 1
  for (int spin = 0; spin < SPIN_MAX; ++spin) {
    fence_c();
    v = __builtin_amdgcn_raw_buffer_load_b64(ws, BELL_R + ((lane & 31) < NM ? (lane & 31) : 0) * 8, 0, AUX_SC1);
    if (__builtin_amdgcn_ballot_w64(v.x != tag) == 0ull) break;
    __builtin_amdgcn_s_sleep(16);
  }
  fence_c();
  float r = row_max16((float)(int)(v.y - ta));
  auto a = __builtin_amdgcn_permlane16_swap(f2u(r), f2u(r), false, false);
  const int d = __builtin_amdgcn_readfirstlane((int)vmax(u2f(a[0]), u2f(a[1]))) + 5;
  return d > 0 ? ta + (u32)d : ta;
}
DI u32 next_q_time(rsrc_t ws, u32 bellm, u32 tag, int tM0, int after, int lane) {
  const u32 ta = bell_time<8>(ws, bellm, tag, lane) + tM0;
  return anchor_time(ws, ta, ta + after - 60, tag, lane) + after;
}
DI void wait_bell32(rsrc_t ws, u32 off, u32 tag, int ta, int tb, int lane) {
  v2u v = {0u, 0u};
#pragma unroll 1
  for (int spin = 0; spin < SPIN_MAX * 4; ++spin) {
    fence_c();
    v = __builtin_amdgcn_raw_buffer_load_b64(ws, off + (lane & 31) * 8, 0, AUX_SC1);
    if (__builtin_amdgcn_ballot_w64(v.x != tag) == 0ull) break;
    __builtin_amdgcn_s_sleep(D_POLL);
  }
  fence_c();
  const u32 t0 = __builtin_amdgcn_readfirstlane(v.y);
  float r = (float)((int)(v.y - t0) + ((lane & 16) ? tb : ta));
  r = row_max16(r);
  auto a = __builtin_amdgcn_permlane16_swap(f2u(r), f2u(r), false, false);
  r = vmax(u2f(a[0]), u2f(a[1]));
  wait_until(t0 + (u32)__builtin_amdgcn_readfirstlane((int)r));
}
DI int my_half() { return (int)((__builtin_amdgcn_s_getreg(20 | (3 << 11)) >> 2) & 1u); }
DI u32 near_copy(rsrc_t ws, u32 tag, int tries) {
  v2u v;
#pragma unroll 1
  for (;;) {
    fence_c();
    v = __builtin_amdgcn_raw_buffer_load_b64(ws, BELL_C, 0, AUX_SC1);
    if ((u32)__builtin_amdgcn_readfirstlane(v.x) == tag || --tries <= 0) break;
    __builtin_amdgcn_s_sleep(32);
  }
  const u32 c = __builtin_amdgcn_readfirstlane(v.x) == tag ? __builtin_amdgcn_readfirstlane(v.y) & 1u : 0u;
  return (c ^ (u32)my_half()) * CP;
}
template <class V> DI void store2(V v, rsrc_t ws, u32 off, u32 soff, u32 rc);
template <> DI void store2<u32>(u32 v, rsrc_t ws, u32 off, u32 soff, u32 rc) { __builtin_amdgcn_raw_buffer_store_b32(v, ws, off, soff + rc, AUX_SC1); __builtin_amdgcn_raw_buffer_store_b32(v, ws, off, soff + (rc ^ CP), AUX_SC1); }
template <> DI void store2<v2u>(v2u v, rsrc_t ws, u32 off, u32 soff, u32 rc) { __builtin_amdgcn_raw_buffer_store_b64(v, ws, off, soff + rc, AUX_SC1); __builtin_amdgcn_raw_buffer_store_b64(v, ws, off, soff + (rc ^ CP), AUX_SC1); }
template <> DI void store2<v4u>(v4u v, rsrc_t ws, u32 off, u32 soff, u32 rc) { __builtin_amdgcn_raw_buffer_store_b128(v, ws, off, soff + rc, AUX_SC1); __builtin_amdgcn_raw_buffer_store_b128(v, ws, off, soff + (rc ^ CP), AUX_SC1); }
DI void ring(rsrc_t ws, u32 off, u32 tag) { __builtin_amdgcn_raw_buffer_store_b64(v2u{tag, now32()}, ws, off, 0, AUX_SC1); }
template <int N> DI void nap() { if (N > 0) __builtin_amdgcn_s_sleep(N); }
template <int NR, bool EXTRA> DI float gather_rows(rsrc_t ws, u32 soff, u32 off, u32 xoff, float base, int& nspin) {
  constexpr int NI = NR / 4;
  static_assert(NR % 4 == 0, "rows come in fours");
  float r;
  int spin = 0;
#pragma unroll 1
  for (;;) {
    fence_c();
    if (EXTRA) base = u2f(__builtin_amdgcn_raw_buffer_load_b32(ws, xoff, soff, AUX_SC1));
    v4u v[NI];
#pragma unroll
    for (int i = 0; i < NI; ++i) { v[i] = __builtin_amdgcn_raw_buffer_load_b128(ws, off + i * CH, soff, AUX_SC1); pin(); }
    f4 acc = __builtin_bit_cast(f4, v[0]);
#pragma unroll
    for (int i = 1; i < NI; ++i) { acc += __builtin_bit_cast(f4, v[i]); asm("" : "+v"(acc)); }
    r = base + red4(acc.x, acc.y, acc.z, acc.w);
    if (__builtin_expect(!any_nan(r), 1)) break;
    if (++spin >= SPIN_MAX) break;
    nap<D_RETRY>();
  }
  fence_c();
  nspin = spin + 1;
  return r;
}

DI void role_A(const void* Kp, const void* Vp, char* wsp, lds_t L, int g, int sp, int lane, int wave) {
  STAMP_DECL;
  TUNE_DECL;
  TUNE_USE;
  STAMP(31);
  const rsrc_t ws = mk_rsrc(wsp, WS_END);
  const size_t kvoff = ((size_t)g * SK + (SK - WIN) + sp * 128) * (DH * 2);
  const v2u ev = __builtin_amdgcn_raw_buffer_load_b64(ws, WS_E, 0, AUX_SC1);
  const int g4 = lane >> 4, i16 = lane & 15;
  const u32 t_in = now32();
  if (wave < 4) {
    const rsrc_t rk = mk_rsrc((const char*)Kp + kvoff + wave * 16384, 16384);
    wait_until(t_in + tKD);
    const u32 hi = lane >> 5, sl = (lane & 31) ^ hi;
#pragma unroll
    for (int u = 0; u < 16; ++u)
      __builtin_amdgcn_raw_ptr_buffer_load_lds(rk, (LAS void*)(L + wave * 16384 + u * 1024), 16, hi * 512 + ((sl ^ ((2 * u) & 14)) * 16), u * 1024, 0, LD_AUX);
  } else {
    const rsrc_t rv = mk_rsrc((const char*)Vp + kvoff, 128 * 512);
    if (wave == 4) {
      const v4u s4 = {SENT, SENT, SENT, SENT};
#pragma unroll
      for (int l2 = 0; l2 < 2; ++l2) {
        const u32 o = WS_O + l2 * O_L + (g * 4 + (sp >> 2)) * CH + (sp & 3) * 2048 + lane * 16;
        store2(s4, ws, o, 0, 0);
        store2(s4, ws, o + 1024, 0, 0);
      }
      store2(s4, ws, lane < 2 ? WS_ST + lane * ST_L + g * 512 + sp * 32 : OOB, 0, 0);
    }
    wait_until(t_in + tKD + tVD);
    pin();
    stage128<16>(rv, (wave - 4) * 128, 512, L, 65536 + (wave - 4) * 16384, 0, lane);
  }
  pin();
  *(LAS v2u*)(L + L_ZERO + lane * 8) = v2u{0u, 0u};
  int idx = (lane & 1) ? 0xEEEE : 0x4444;
  int paddr = (((lane >> 2) & 3) == g4) ? L_OA + (g4 * 16 + (lane & 3) * 4) * 4 : L_ZERO;
  int saddr = L_STAT + ((i16 >> 3) * 8 + (i16 & 4) + ((i16 + g4) & 3)) * 4;
  const u32 bellm = BELL_M + (((blockIdx.x >> 3) & 1) * 8 + g) * 128;
  const int stripe = wave & 3, t0 = wave < 4 ? 2 : 0;
  u32 ooff = WS_O + (g * 4 + (sp >> 2)) * CH + (sp & 3) * 2048 + (lane >> 5) * 1024 + (64 * stripe + 16 * t0 + (lane & 31)) * 4;
  int vb0 = 65536 + stripe * 16384 + tr_base128(t0, lane), vb1 = 65536 + stripe * 16384 + tr_base128(t0 + 1, lane);
  u32 qoff = WS_Q + (g >> 2) * CH + (g & 3) * 2048 + lane * 16;
  const int qp = L_QP + (wave & 3) * 1024;
  const int gq = (lane >> 2) & 3, qrel = ((gq & 1) * 128 + 8 * g4 + 4 * (lane & 1)) * 4;
  int qa = gq < 2 ? qp + qrel : L_ZERO, qb = gq >= 2 ? qp + qrel : L_ZERO;
  int qw = qp + lane * 8;
  int pw = (lane & 1) ? L_DUM + (lane & 31) * 4 : L_OA + ((2 * (wave & 3) + (g4 >> 1)) * 16 + (g4 & 1) * 8 + ((lane >> 1) & 1) * 4 + (i16 >> 2)) * 4;
  int sw = i16 != 1 ? L_DUM + (lane & 31) * 4 : L_STAT + ((g4 & 1) * 8 + 2 * (wave & 3) + (g4 >> 1)) * 4;
  u32 sto = lane == 0 ? WS_ST + g * 512 + sp * 32 : OOB;
  keep(idx); keep(paddr); keep(saddr); keep(ooff); keep(vb0); keep(vb1); keep(qoff); keep(qa); keep(qb); keep(qw); keep(pw); keep(sw); keep(sto);
  const u64 E = ((u64)(u32)__builtin_amdgcn_readfirstlane(ev.y) << 32) | (u32)__builtin_amdgcn_readfirstlane(ev.x);
  STAMP(0);
  asm volatile("s_waitcnt vmcnt(0)" ::: "memory");
  STAMP(1);
  v8u kf[8], vf[4];
  if (wave < 4) {
    const int rb = wave * 16384 + i16 * 512;
#pragma unroll
    for (int hs = 0; hs < 4; ++hs) {
      const int a = rb + (((g4 ^ (i16 & 3)) | ((hs ^ (i16 >> 2)) << 2)) * 16);
#pragma unroll
      for (int tt = 0; tt < 2; ++tt)
#pragma unroll
        for (int s2 = 0; s2 < 2; ++s2) {
          const v4u v = lds128(L, a + tt * 8192 + s2 * 256);
          v8u& f = kf[tt * 4 + 2 * s2 + (hs >> 1)];
          if (hs & 1) { f[4] = v.x; f[5] = v.y; f[6] = v.z; f[7] = v.w; } else { f[0] = v.x; f[1] = v.y; f[2] = v.z; f[3] = v.w; }
        }
    }
#pragma unroll
    for (int i = 0; i < 4; ++i) vf[i] = v8u{0, 0, 0, 0, 0, 0, 0, 0};
  } else {
    if (wave == 4 && lane == 0) ring(ws, BELL_W + g * 256 + 128 + sp * 8, (u32)E + 1);
    vf[0] = frag128<0>(L, vb0); vf[1] = frag128<0>(L, vb1); vf[2] = frag128<8192>(L, vb0); vf[3] = frag128<8192>(L, vb1);
#pragma unroll
    for (int i = 0; i < 8; ++i) kf[i] = v8u{0, 0, 0, 0, 0, 0, 0, 0};
  }
  u32 rc = 0;
  constexpr float C2 = 0.0625f * 1.44269504088896f;
  const v8u ones = {0x3f803f80u, 0x3f803f80u, 0x3f803f80u, 0x3f803f80u, 0x3f803f80u, 0x3f803f80u, 0x3f803f80u, 0x3f803f80u};
#pragma unroll 1
  for (int layer = 0; layer < 2; ++layer) {
    const u32 tag = (u32)E + 1 + layer;
    f4 d00 = {0.f, 0.f, 0.f, 0.f}, d10 = d00, d01 = d00, d11 = d00, l0 = d00, l1 = d00;
    keep(d00); keep(d10); keep(d01); keep(d11); keep(l0); keep(l1);
    if (wave < 4) {
      f4 d = {0.f, 0.f, 0.f, 0.f};
      keep(d);
      {
        u32 tgt;
        if (wave == 0) {
          rc = layer ? near_copy(ws, (u32)E + 1, 4) : (u32)my_half() * CP; *(LAS u32*)(L + L_TGT + 8) = rc;
          tgt = layer ? next_q_time(ws, bellm, (u32)E + 1, tM0, tB + tA, lane) : bell_time<8>(ws, BELL_A0 + g * 128, tag, lane) + tA0;
          *(LAS v2u*)(L + L_TGT) = v2u{tag, tgt};
        } else {
          v2u w;
#pragma unroll 1
          for (int spin = 0; spin < SPIN_MAX * 64; ++spin) { fence_c(); w = *(volatile LAS v2u*)(L + L_TGT); if ((u32)__builtin_amdgcn_readfirstlane(w.x) == tag) break; __builtin_amdgcn_s_sleep(1); }
          tgt = __builtin_amdgcn_readfirstlane(w.y);
          rc = __builtin_amdgcn_readfirstlane(*(volatile LAS u32*)(L + L_TGT + 8));
        }
        wait_until(tgt);
      }
      STAMP_L(2);
      int spin = 0;
#pragma unroll 1
      for (;;) {
        fence_c();
        const v4u a = __builtin_amdgcn_raw_buffer_load_b128(ws, qoff, layer * Q_L + rc, AUX_SC1);
        const v4u b = __builtin_amdgcn_raw_buffer_load_b128(ws, qoff + 1024, layer * Q_L + rc, AUX_SC1);
        pin();
        *(LAS v2u*)(L + qw) = v2u{a.x, a.z};
        *(LAS v2u*)(L + qw + 512) = v2u{b.x, b.z};
        if (__builtin_expect(__builtin_amdgcn_ballot_w64((((a.y ^ tag) | (a.w ^ tag)) | ((b.y ^ tag) | (b.w ^ tag))) != 0u) == 0ull, 1)) break;
        if (++spin >= SPIN_MAX) break;
        nap<D_RETRY>();
      }
      fence_c();
      if (layer == 0 && wave == 3 && lane == 0) ring(ws, BELL_W + g * 256 + sp * 8, tag);
      RETRY(0, spin + 1);
      STAMP_L(3);
      v4u qf[8];
#pragma unroll
      for (int s = 0; s < 4; ++s) qf[s] = lds128(L, qa + s * 128);
#pragma unroll
      for (int s = 0; s < 4; ++s) qf[4 + s] = lds128(L, qb + s * 128);
      pin();
#pragma unroll
      for (int s = 0; s < 8; ++s) d = smf(qf[s], kf[s], d, idx);
      const float t = opq(d.x + d.y);
      const float mt = row_max16(t) * C2;
      const float p = __builtin_amdgcn_exp2f(t * C2 - mt);
      *(LAS float*)(L + sw) = mt;
      *(LAS u32*)(L + pw) = pk_bf16(p, dpp<XOR1>(p));
      STAMP_L(4);
    }
    bar();
    const v4u pa0 = lds128(L, paddr), pa1 = lds128(L, paddr + 256);
    const float ms = *(LAS float*)(L + saddr);
    if (wave >= 4) rc = __builtin_amdgcn_readfirstlane(*(LAS u32*)(L + L_TGT + 8));
    if (layer == 0 && wave < 4) { vf[0] = frag128<0>(L, vb0); vf[1] = frag128<0>(L, vb1); vf[2] = frag128<8192>(L, vb0); vf[3] = frag128<8192>(L, vb1); }
    pin();
    d00 = smf(pa0, vf[0], d00, idx); d10 = smf(pa0, vf[1], d10, idx); d01 = smf(pa1, vf[2], d01, idx); d11 = smf(pa1, vf[3], d11, idx);
    if (wave == 7) { l0 = smf(pa0, ones, l0, idx); l1 = smf(pa1, ones, l1, idx); }
    const float M = max8(ms);
    const float e = __builtin_amdgcn_exp2f(ms - M);
    const float o00 = opq(dpp<NBC + 0>(e) * opq(d00.x + d00.y) + dpp<NBC + 4>(e) * opq(d01.x + d01.y));
    const float o10 = opq(dpp<NBC + 0>(e) * opq(d10.x + d10.y) + dpp<NBC + 4>(e) * opq(d11.x + d11.y));
    const float o01 = opq(dpp<NBC + 8>(e) * opq(d00.z + d00.w) + dpp<NBC + 12>(e) * opq(d01.z + d01.w));
    const float o11 = opq(dpp<NBC + 8>(e) * opq(d10.z + d10.w) + dpp<NBC + 12>(e) * opq(d11.z + d11.w));
    store2(f2u(red4(o00, o10, o01, o11)), ws, ooff, layer * O_L, rc);
    if (wave == 7) {
      const float a0 = dpp<NBC + 0>(e) * (l0.x + l0.y) + dpp<NBC + 4>(e) * (l1.x + l1.y), a1 = dpp<NBC + 8>(e) * (l0.z + l0.w) + dpp<NBC + 12>(e) * (l1.z + l1.w);
      auto x = __builtin_amdgcn_permlane16_swap(f2u(a0), f2u(a1), false, false); const float u = u2f(x[0]) + u2f(x[1]);
      auto y = __builtin_amdgcn_permlane32_swap(f2u(u), f2u(u), false, false); const float Ls = u2f(y[0]) + u2f(y[1]);
      auto z = __builtin_amdgcn_permlane16_swap(f2u(Ls), f2u(Ls), false, false);
      store2(v4u{f2u(M), z[0], f2u(dpp<ROR8>(M)), z[1]}, ws, sto, layer * ST_L, rc);
    }
    STAMP_L(5);
  }
  STAMP_OUT;
}

DI void role_B(const void* xp, const void* wqp, const void* wop, char* wsp, lds_t L, int g, int c, int lane, int wave) {
  STAMP_DECL;
  TUNE_DECL;
  TUNE_USE;
  STAMP(31);
  const rsrc_t ws = mk_rsrc(wsp, WS_END);
  const u32 t_in = now32();
  const rsrc_t rq = mk_rsrc(wqp, D * 4096 * 2), ro = mk_rsrc(wop, 4096 * D * 2), rx = mk_rsrc(xp, D * 2);
  const v2u ev = __builtin_amdgcn_raw_buffer_load_b64(ws, WS_E, 0, AUX_SC1);
  const int i16 = lane & 15, g4 = lane >> 4;
  const u32 x0 = __builtin_amdgcn_raw_buffer_load_b16(rx, (wave < 7 ? wave * 64 + 4 * i16 + g4 : 0) * 2, 0, 0);
  if (wave == 0) {
    const v4u s4 = {SENT, SENT, SENT, SENT};
    store2(s4, ws, c < 7 && lane < 32 ? WS_X1 + g4 * X1_L + (g >> 2) * CH + (g & 3) * (D * 4) + c * 256 + i16 * 16 : OOB, 0, 0);
  }
  pin();
  stage128<7, WQ_AUX>(rq, (g * 512 + c * 64) * 2, 8192, L, L_IMG0, wave * 7, lane);
  pin();
  if (wave == 7) {
#pragma unroll
    for (int i = 0; i < 4; ++i) *(LAS u32*)(L + L_ONE + (i * 64 + lane) * 4) = 0x3f803f80u;
    *(LAS u32*)(L + L_ZERO + lane * 4) = 0u;
  }
  int trb = tr_base128(wave & 3, lane);
  int idx = (lane & 1) ? 0xEEEE : 0x4444;
  const int aoff = (8 * g4 + 4 * (lane & 1)) * 4;
  int ab = wave < 4 ? ((lane & 2) ? L_ONE : L_XA) + aoff : L_OA + aoff;
  int sa = lane < 56 ? L_XA + lane * 16 : L_ZERO;
  int xw = put_A_addr(L_XA, wave, lane), ow = put_A_addr(L_OA, wave, lane);
  const u32 bellm = BELL_M + (((blockIdx.x >> 3) & 1) * 8 + g) * 128;
  const int h = wave >> 2;
  u32 so = WS_ST + g * 512 + h * 8 + (4 * (lane & 3) + ((g4 + (i16 >> 2)) & 3)) * 32;
  u32 oo = WS_O + g * 4 * CH + g4 * 2048 + h * 1024 + (wave & 3) * 256 + i16 * 16;
  u32 qpos = (lane & 49) ? OOB : WS_Q + (g >> 2) * CH + (g & 3) * 2048 + (c * 32 + 8 * (wave & 3) + 4 * ((lane >> 1) & 1) + (i16 >> 2)) * 8;
  u32 x1o = lane < 16 ? WS_X1 + (g >> 2) * CH + (g & 3) * (D * 4) + (c * 64 + (wave & 3) * 16 + lane) * 4 : OOB;
  u32 mo = g4 * (D * 4) + wave * 256 + i16 * 16, mx = 7 * CH + (wave * 64 + lane) * 4;
  keep(trb); keep(idx); keep(ab); keep(sa); keep(xw); keep(ow); keep(so); keep(oo); keep(qpos); keep(x1o); keep(mo); keep(mx);
  const u64 E = ((u64)(u32)__builtin_amdgcn_readfirstlane(ev.y) << 32) | (u32)__builtin_amdgcn_readfirstlane(ev.x);
  const u64 T = E + 2;
  if (wave < 7) put_A(L, xw, u2f(x0 << 16));
  STAMP(0);
  asm volatile("s_waitcnt vmcnt(0)" ::: "memory");
  bar();
  if (wave == 7 && lane == 0) ring(ws, BELL_A0 + g * 128 + c * 8, (u32)E + 1);
  STAMP(1);
  v8u fr[8];
  if (wave < 4) {
    fr[0] = frag128<0>(L, L_IMG0 + trb); fr[1] = frag128<8192>(L, L_IMG0 + trb); fr[2] = frag128<2 * 8192>(L, L_IMG0 + trb); fr[3] = frag128<3 * 8192>(L, L_IMG0 + trb);
    fr[4] = frag128<4 * 8192>(L, L_IMG0 + trb); fr[5] = frag128<5 * 8192>(L, L_IMG0 + trb); fr[6] = frag128<6 * 8192>(L, L_IMG0 + trb);
    fr[7] = fr[6];
  } else {
#pragma unroll
    for (int i = 0; i < 8; ++i) fr[i] = v8u{0, 0, 0, 0, 0, 0, 0, 0};
  }
  u32 rc = (u32)my_half() * CP;
#pragma unroll 1
  for (int layer = 0; layer < 2; ++layer) {
    const u32 tag = (u32)E + 1 + layer;
    u32 tq = 0;
    f4 d = {0.f, 0.f, 0.f, 0.f};
    if (layer > 0) {
      if (wave == 7) { wait_ready(ws, T, lane); tq = next_q_time(ws, bellm, (u32)E + 1, tM0, tB, lane); wait_until(tq); }
      keep(d);
      bar();
      STAMP(12);
      if (wave < 7) {
        int ns;
        put_A(L, xw, gather_rows<NM, true>(ws, WS_MP + rc, mo, mx, 0.f, ns));
        RETRY(0, ns);
      }
      STAMP(13);
      bar();
    }
    if (wave < 4) {
      v4u a[7];
#pragma unroll
      for (int s = 0; s < 7; ++s) a[s] = lds128(L, ab + s * 128);
      const v4u sv = lds128(L, sa);
      pin();
#pragma unroll
      for (int s = 0; s < 7; ++s) d = smf(a[s], fr[s], d, idx);
      float mean, rstd;
      ln_stats(sv, mean, rstd);
      const float q = rstd * (opq(d.x + d.y) - mean * opq(d.z + d.w));
      store2(v2u{pk_bf16(q, dpp<XOR1>(q)), tag}, ws, qpos, layer * Q_L, rc);
    }
    STAMP_L(4);
    if (layer == 0) {
      wait_until(t_in + tWO);
      if (c < 7) stage128<8, WO_AUX>(ro, g * 512 * (D * 2) + c * 128, D * 2, L, L_IMG1, wave * 8, lane);
      asm volatile("s_waitcnt vmcnt(0)" ::: "memory");
      if (wave == 7) *(LAS u32*)(L + L_TGT) = near_copy(ws, tag, 6);
      bar();
      rc = __builtin_amdgcn_readfirstlane(*(LAS u32*)(L + L_TGT));
      if (wave >= 4 && c < 7) {
        fr[0] = frag128<0>(L, L_IMG1 + trb); fr[1] = frag128<8192>(L, L_IMG1 + trb); fr[2] = frag128<2 * 8192>(L, L_IMG1 + trb); fr[3] = frag128<3 * 8192>(L, L_IMG1 + trb);
        fr[4] = frag128<4 * 8192>(L, L_IMG1 + trb); fr[5] = frag128<5 * 8192>(L, L_IMG1 + trb); fr[6] = frag128<6 * 8192>(L, L_IMG1 + trb); fr[7] = frag128<7 * 8192>(L, L_IMG1 + trb);
      }
    }
    if (wave == 7) {
      if (layer == 0) wait_bell32(ws, BELL_W + g * 256, tag, tW0, tWV, lane);
      else if (c < 7) wait_until(tq + tA + tW1);
      if (layer == 0 && c == 7 && lane < 16) ring(ws, BELL_M + lane * 128 + g * 8, tag);
    }
    if (c == 7) continue;
    f4 dw = {0.f, 0.f, 0.f, 0.f};
    keep(dw);
    bar();
    STAMP_L(5);
    {
      float ov;
      int spin = 0;
#pragma unroll 1
      for (;;) {
        fence_c();
        const v2u ml = __builtin_amdgcn_raw_buffer_load_b64(ws, so, layer * ST_L + rc, AUX_SC1);
        const float m = u2f(ml.x), l = u2f(ml.y);
        v4u o[4];
#pragma unroll
        for (int i = 0; i < 4; ++i) { o[i] = __builtin_amdgcn_raw_buffer_load_b128(ws, oo + i * CH, layer * O_L + rc, AUX_SC1); pin(); }
        const float e = __builtin_amdgcn_exp2f(m - row_max16(m));
        const float inv = __builtin_amdgcn_rcpf(row_sum16(e * l));
        float a0 = dpp<NBC + 0>(e) * u2f(o[0].x), a1 = dpp<NBC + 0>(e) * u2f(o[0].y), a2 = dpp<NBC + 0>(e) * u2f(o[0].z), a3 = dpp<NBC + 0>(e) * u2f(o[0].w);
        a0 = opq(a0); a1 = opq(a1); a2 = opq(a2); a3 = opq(a3);
#define OACC(i) a0 = opq(a0 + dpp<NBC + i>(e) * u2f(o[i].x)); a1 = opq(a1 + dpp<NBC + i>(e) * u2f(o[i].y)); a2 = opq(a2 + dpp<NBC + i>(e) * u2f(o[i].z)); a3 = opq(a3 + dpp<NBC + i>(e) * u2f(o[i].w));
        OACC(1) OACC(2) OACC(3)
#undef OACC
        ov = red4(a0, a1, a2, a3) * inv;
        if (__builtin_expect(!any_nan(ov), 1)) break;
        if (++spin >= SPIN_MAX) break;
        nap<D_RETRY>();
      }
      fence_c();
      RETRY(1, spin + 1);
      put_A(L, ow, ov);
    }
    STAMP_L(6);
    bar();
    if (wave >= 4) {
      v4u a[8];
#pragma unroll
      for (int s = 0; s < 8; ++s) a[s] = lds128(L, ab + s * 128);
      pin();
#pragma unroll
      for (int s = 0; s < 8; ++s) dw = smf(a[s], fr[s], dw, idx);
      store2(f2u(dw.x + dw.y), ws, x1o, layer * X1_L, rc ^ CP);
    }
    STAMP_L(7);
  }
  STAMP_OUT;
}

DI void role_M(const void* xp, const void* w1p, const void* w3p, const void* w2p, void* outp, char* wsp, lds_t L, int m, int lane, int wave, u32 t_first) {
  STAMP_DECL;
  TUNE_DECL;
  TUNE_USE;
  STAMP(31);
  const lds_t S = L + (L_SM_M - L_SM);
  const rsrc_t ws = mk_rsrc(wsp, WS_END);
  const rsrc_t r1 = mk_rsrc(w1p, D * FF * 2), r3 = mk_rsrc(w3p, D * FF * 2), r2 = mk_rsrc(w2p, D * FF * 2), rx = mk_rsrc(xp, D * 2);
  const v2u ev = __builtin_amdgcn_raw_buffer_load_b64(ws, WS_E, 0, AUX_SC1);
  const int i16 = lane & 15, g4 = lane >> 4;
  const u32 x0 = __builtin_amdgcn_raw_buffer_load_b16(rx, (wave < 7 ? wave * 64 + 4 * i16 + g4 : 0) * 2, 0, 0);
  if (wave == 0) {
    const v4u s4 = {SENT, SENT, SENT, SENT};
#pragma unroll
    for (int l2 = 0; l2 < 2; ++l2) {
      const u32 o = WS_MP + l2 * MP_L + (m >> 2) * CH + (m & 3) * (D * 4) + lane * 16;
      store2(s4, ws, o, 0, 0);
      store2(s4, ws, lane < 48 ? o + 1024 : OOB, 0, 0);
      if (m == 0) {
        const u32 ox = WS_MP + l2 * MP_L + 7 * CH + lane * 16;
        store2(s4, ws, ox, 0, 0);
        store2(s4, ws, lane < 48 ? ox + 1024 : OOB, 0, 0);
      }
    }
  }
  const u32 t_in = now32();
  const u64 E = ((u64)(u32)__builtin_amdgcn_readfirstlane(ev.y) << 32) | (u32)__builtin_amdgcn_readfirstlane(ev.x);
  const u64 T = E + 2;
  if (wave == 0) {
    asm volatile("s_waitcnt vmcnt(0)" ::: "memory");
    if (lane == 0) { const v2u t = {(u32)T, (u32)(T >> 32)}; __builtin_amdgcn_raw_buffer_store_b64(t, ws, WS_RDY + m * 8, 0, AUX_SC1); }
  }
  wait_until(t_in + tML);
  pin();
  v4u w2a = {0u, 0u, 0u, 0u}, w2b = w2a;
  const u32 chunk2 = (lane & 7) ^ ((((lane >> 4) & 1) << 1) | ((wave & 1) << 2));
  {
    const u32 voff = (u32)(m * 64 + wave * 8 + (lane >> 3)) * (D * 2) + chunk2 * 16;
    stage128<7, WM_AUX>(r1, m * 128, FF * 2, L, L_IMG0, wave * 7, lane);
#pragma unroll
    for (int b = 0; b < 3; ++b) __builtin_amdgcn_raw_ptr_buffer_load_lds(r2, (LAS void*)(L + L_W2 + (b * 8 + wave) * 1024), 16, voff, b * 128, 0, WM_AUX);
    stage128<7, WM_AUX>(r3, m * 128, FF * 2, L, L_IMG1, wave * 7, lane);
#pragma unroll
    for (int b = 3; b < 5; ++b) __builtin_amdgcn_raw_ptr_buffer_load_lds(r2, (LAS void*)(L + L_W2 + (b * 8 + wave) * 1024), 16, voff, b * 128, 0, WM_AUX);
    w2a = __builtin_amdgcn_raw_buffer_load_b128(r2, voff, 5 * 128, WM_AUX);
    w2b = __builtin_amdgcn_raw_buffer_load_b128(r2, voff, 6 * 128, WM_AUX);
  }
  pin();
  if (wave == 7) {
#pragma unroll
    for (int i = 0; i < 4; ++i) *(LAS u32*)(S + L_ONE + (i * 64 + lane) * 4) = 0x3f803f80u;
  }
  *(LAS v2u*)(S + L_ZERO + lane * 8) = v2u{0u, 0u};
  STAMP(0);
  asm volatile("s_waitcnt vmcnt(0)" ::: "memory");
  bar();
  STAMP(1);
  v8u fr[14];
  const int trb = tr_base128(wave & 3, lane);
  if (wave < 4) {
#define FR2(s) fr[s] = frag128<s * 8192>(L, L_IMG0 + trb); fr[7 + s] = frag128<s * 8192>(L, L_IMG1 + trb);
    FR2(0) FR2(1) FR2(2) FR2(3) FR2(4) FR2(5) FR2(6)
#undef FR2
  } else {
#pragma unroll
    for (int i = 0; i < 14; ++i) fr[i] = v8u{0, 0, 0, 0, 0, 0, 0, 0};
  }
  if (wave == 7) *(LAS u32*)(S + L_TGT) = near_copy(ws, (u32)E + 1, 6);
  bar();
  const u32 rc = __builtin_amdgcn_readfirstlane(*(LAS u32*)(S + L_TGT));
  *(LAS v4u*)(L + L_IMG0 + wave * 1024 + lane * 16) = w2a;
  *(LAS v4u*)(L + L_IMG0 + (8 + wave) * 1024 + lane * 16) = w2b;
  bar();
  if (wave >= 4) {
#pragma unroll
    for (int u = 0; u < 7; ++u) {
      const int ct = 7 * (wave - 4) + u, b = ct >> 2;
      fr[u] = frag128<0>(L, (b < 5 ? L_W2 + b * 8192 : L_IMG0 + (b - 5) * 8192) + tr_base128(ct & 3, lane));
    }
  }
  if (wave == 4 && lane == 0) ring(ws, BELL_R + m * 8, (u32)E + 1);
  STAMP(7);
  int idx = (lane & 1) ? 0xEEEE : 0x4444;
  const int aoff = (8 * g4 + 4 * (lane & 1)) * 4, gq = (lane >> 2) & 3;
  int ab = ((lane & 2) ? L_ONE : L_XA) + aoff;
  int sa = lane < 56 ? L_XA + lane * 16 : L_ZERO;
  int xw = put_A_addr(L_XA, wave, lane);
  int hw = (lane & 49) ? L_DUM + (lane & 31) * 4 : L_OA + (8 * (wave & 3) + 4 * ((lane >> 1) & 1) + (i16 >> 2)) * 4;
  int h0 = gq == 0 ? L_OA + aoff : L_ZERO, h1 = gq == 1 ? L_OA + aoff : L_ZERO, h2 = gq == 2 ? L_OA + aoff : L_ZERO, h3 = gq == 3 ? L_OA + aoff : L_ZERO;
  u32 xo = g4 * (D * 4) + wave * 256 + i16 * 16;
  u32 po = WS_MP + (m >> 2) * CH + (m & 3) * (D * 4) + (112 * (wave & 3) + lane) * 4, po2 = lane < 48 ? po + 256 : OOB;
  u32 x1o = m == 0 ? WS_MP + 7 * CH + (wave * 64 + lane) * 4 : OOB;
  keep(idx); keep(ab); keep(sa); keep(xw); keep(hw); keep(h0); keep(h1); keep(h2); keep(h3); keep(xo); keep(po); keep(po2); keep(x1o);
  float xres = u2f(x0 << 16);
  const u32 bellm = BELL_M + (((blockIdx.x >> 3) & 1) * 8 + (blockIdx.x & 7)) * 128;
  u32 tan = 0;
#pragma unroll 1
  for (int layer = 0; layer < 2; ++layer) {
    const u32 tag = (u32)E + 1 + layer;
    if (wave == 7) { if (layer == 0) { wait_ready(ws, T, lane); tan = bell_time<8>(ws, bellm, tag, lane) + tM0; } else tan = anchor_time(ws, tan, tan + tB + tA, (u32)E + 1, lane) + (tB + tA + tW1 + tM1); wait_until(tan); }
    f4 da = {0.f, 0.f, 0.f, 0.f}, db = da;
    keep(da); keep(db);
    bar();
    STAMP_L(2);
    if (wave < 7) {
      int ns;
      const u32 dx = put_A(S, xw, gather_rows<8, false>(ws, WS_X1 + layer * X1_L + rc, xo, 0, xres, ns));
      RETRY(0, ns);
      xres = own_half(dx, lane);
      store2(f2u(xres), ws, x1o, layer * MP_L, rc ^ CP);
    }
    STAMP_L(3);
    bar();
    if (wave < 4) {
      v4u a[7];
#pragma unroll
      for (int s = 0; s < 7; ++s) a[s] = lds128(S, ab + s * 128);
      const v4u sv = lds128(S, sa);
      pin();
#pragma unroll
      for (int s = 0; s < 7; ++s) { da = smf(a[s], fr[s], da, idx); db = smf(a[s], fr[7 + s], db, idx); }
      float mean, rstd;
      ln_stats(sv, mean, rstd);
      const float gt = rstd * (opq(da.x + da.y) - mean * opq(da.z + da.w));
      const float up = rstd * (opq(db.x + db.y) - mean * opq(db.z + db.w));
      const float hv = gt * __builtin_amdgcn_rcpf(1.0f + __builtin_amdgcn_exp2f(gt * -1.44269504088896f)) * up;
      *(LAS u32*)(S + hw) = pk_bf16(hv, dpp<XOR1>(hv));
    }
    STAMP_L(4);
    bar();
    u32 th = 0;
    if (wave == m) th = now32();
    if (wave >= 4) {
      const v4u a0 = lds128(S, h0), a1 = lds128(S, h1), a2 = lds128(S, h2), a3 = lds128(S, h3);
      pin();
      da = smf(a0, fr[0], da, idx); db = smf(a0, fr[4], db, idx);
      da = smf(a1, fr[1], da, idx); db = smf(a1, fr[5], db, idx);
      da = smf(a2, fr[2], da, idx); db = smf(a2, fr[6], db, idx);
      da = smf(a3, fr[3], da, idx);
      const u32 y0 = f2u(da.x + da.y), y1 = f2u(db.x + db.y);
      __builtin_amdgcn_raw_buffer_store_b32(y0, ws, po, layer * MP_L + (rc ^ CP), AUX_SC1);
      __builtin_amdgcn_raw_buffer_store_b32(y1, ws, po2, layer * MP_L + (rc ^ CP), AUX_SC1);
      __builtin_amdgcn_raw_buffer_store_b32(y0, ws, po, layer * MP_L + rc, AUX_SC1);
      __builtin_amdgcn_raw_buffer_store_b32(y1, ws, po2, layer * MP_L + rc, AUX_SC1);
    }
    STAMP_L(5);
    if (layer == 0) {
      if (wave < 7) { nap<50>(); int ns; xres = rbf(gather_rows<NM, false>(ws, WS_MP + rc, xo, 0, xres, ns)); RETRY(1, ns); }
    } else if (m < 7 && wave == m) {
      int ns;
      wait_until(th + tF);
      const float y = gather_rows<NM, false>(ws, WS_MP + MP_L + rc, xo, 0, xres, ns); RETRY(1, ns);
      auto sw = __builtin_amdgcn_permlane16_swap(f2u(y), f2u(y), false, false);
      const u32 dw = pk_bf16(u2f(sw[0]), u2f(sw[1]));
      if ((lane & 16) == 0) ((u32*)outp)[32 * m + 2 * i16 + (lane >> 5)] = dw;
      if (m == 0 && lane == 0) __builtin_amdgcn_raw_buffer_store_b64(v2u{(u32)T, (u32)(T >> 32)}, ws, WS_E, 0, AUX_SC1);
    }
    STAMP_L(6);
  }
  STAMP_OUT;
}

DI void role_P(char* wsp, lds_t L, int lane, int wave) {
  const rsrc_t ws = mk_rsrc(wsp, WS_END);
  const u32 tag = __builtin_amdgcn_raw_buffer_load_b32(ws, WS_E, 0, AUX_SC1) + 1;
  const u32 off = lane == 0 ? WS_PROBE + wave * 128 : OOB;
  u32 best[2] = {~0u, ~0u};
#pragma unroll 1
  for (int r = 0; r < 3; ++r) {
    __builtin_amdgcn_raw_buffer_store_b32(tag, ws, off, 0, AUX_SC1);
    __builtin_amdgcn_raw_buffer_store_b32(tag, ws, off, CP, AUX_SC1);
    asm volatile("s_waitcnt vmcnt(0)" ::: "memory");
#pragma unroll
    for (int c = 0; c < 2; ++c) {
      const u64 t0 = __builtin_amdgcn_s_memtime();
      asm volatile("s_waitcnt lgkmcnt(0)" ::: "memory");
      u32 v = __builtin_amdgcn_raw_buffer_load_b32(ws, off, c * CP, AUX_SC1);
      asm volatile("s_waitcnt vmcnt(0)" : "+v"(v) :: "memory");
      const u32 dt = (u32)(__builtin_amdgcn_s_memtime() - t0);
      best[c] = dt < best[c] ? dt : best[c];
    }
  }
  if (lane == 0) *(LAS int*)(L + wave * 4) = (int)best[1] - (int)best[0];
  bar();
  if (wave == 0 && lane == 0) {
    int d = 0;
#pragma unroll
    for (int w = 0; w < 8; ++w) { const int x = *(LAS int*)(L + w * 4); d += x < -300 ? -300 : x > 300 ? 300 : x; }
    __builtin_amdgcn_raw_buffer_store_b64(v2u{tag, (d < 0 ? 1u : 0u) ^ (u32)my_half()}, ws, BELL_C, 0, AUX_SC1);
  }
}
DI void role_H(const void* w3p, const void* w2p, char* wsp, lds_t L, int m, int lane, int wave) {
  TUNE_DECL;
  TUNE_USE;
  const rsrc_t r3 = mk_rsrc(w3p, D * FF * 2), r2 = mk_rsrc(w2p, D * FF * 2);
  wait_until(now32() + tML - tHL);
  stage128<7, 0>(r3, m * 128, FF * 2, L, 0, wave * 7, lane);
  const u32 voff = (u32)(m * 64 + wave * 8 + (lane >> 3)) * (D * 2) + (lane & 7) * 16;
#pragma unroll
  for (int b = 3; b < 7; ++b) __builtin_amdgcn_raw_ptr_buffer_load_lds(r2, (LAS void*)(L + 65536 + (b * 8 + wave) * 1024), 16, voff, b * 128, 0, 0);
  asm volatile("s_waitcnt vmcnt(0)" ::: "memory");
}

__global__ void __launch_bounds__(NTHR, 2) fused_decode_kernel(const void* xp, const void* Kp, const void* Vp, const void* wqp, const void* wop,
                                                               const void* w1p, const void* w3p, const void* w2p, void* outp, char* wsp) {
  extern __shared__ __attribute__((aligned(16))) char smem[];
  const lds_t L = (lds_t)smem;
  const int lane = threadIdx.x & 63, wave = __builtin_amdgcn_readfirstlane(threadIdx.x >> 6);
  const int g = blockIdx.x & 7, j = blockIdx.x >> 3;
  if (j == 0) {
    u64 pc; int len;
    asm volatile("s_getpc_b64 %0\n.Lfd_pc:\n\ts_mov_b32 %1, .Lfd_end-.Lfd_pc" : "=s"(pc), "=s"(len));
    const rsrc_t rc = mk_rsrc((const void*)pc, len < 0 ? 0u : (u32)len);
    __builtin_amdgcn_raw_ptr_buffer_load_lds(rc, (LAS void*)(L + L_PF), 4, (u32)(wave * 2048 + lane * 32), 0, 0, 0);
    __builtin_amdgcn_raw_ptr_buffer_load_lds(rc, (LAS void*)(L + L_PF), 4, (u32)(16384 + wave * 2048 + lane * 32), 0, 0, 0);
  }
  if (j < JA) role_B(xp, wqp, wop, wsp, L, g, j, lane, wave);
  else if (j < JM) role_A(Kp, Vp, wsp, L, g, j - JA, lane, wave);
  else if (g < 7 && j < JH) role_M(xp, w1p, w3p, w2p, outp, wsp, L, g * 4 + (j - JM), lane, wave, 0u);
  else if (blockIdx.x == NWG - 1) role_P(wsp, L, lane, wave);
  else if (g < 7) role_H(w3p, w2p, wsp, L, g * 4 + (j - JH), lane, wave);
  asm volatile(".Lfd_end:");
}

extern "C" void kernel_launch(void* const* d_in, const int* in_sizes, int n_in, void* d_out, int out_size, void* d_ws, size_t ws_size, hipStream_t stream) {
  (void)hipFuncSetAttribute((const void*)fused_decode_kernel, hipFuncAttributeMaxDynamicSharedMemorySize, LDS_BYTES);
  fused_decode_kernel<<<NWG, NTHR, LDS_BYTES, stream>>>(d_in[0], d_in[1], d_in[2], d_in[3], d_in[4], d_in[6], d_in[7], d_in[8], d_out, (char*)(((size_t)d_ws + (1u << 21) - 1) & ~(size_t)((1u << 21) - 1)));
}
```
